```python
import torch

def _softmax(x, axis=-1):
    return torch.softmax(x, dim=axis)
def _gelu(x):
    return torch.nn.functional.gelu(x, approximate="tanh")
def _silu(x):
    return torch.nn.functional.silu(x)
def _sigmoid(x):
    return torch.sigmoid(x)
def _layernorm(x, axis=-1, eps=1e-5):
    m = x.mean(dim=axis, keepdim=True)
    v = x.var(dim=axis, keepdim=True, unbiased=False)
    return (x - m) / torch.sqrt(v + eps)
def _scatter_add(tgt, idx, src, axis=0):
    return tgt.clone().index_add_(axis, idx, src)
def _segment_sum(src, seg, n):
    out = torch.zeros((n,) + src.shape[1:], dtype=src.dtype)
    return out.index_add_(0, seg, src)
def _pad(x, pw):
    flat = tuple(v for pair in reversed(pw) for v in pair)
    return torch.nn.functional.pad(x, flat)

kernel_name = 'interleave_layers__linear_attn_block__dense_activation_silu__prompt'

def setup_inputs():

    _w = (lambda a: (lambda t: t.to(torch.bfloat16) if t.is_floating_point() else t)(torch.as_tensor(a)))
    def _A_setup():
        B, S, D = 1, 2048, 512
        return {
            "x": _w((lambda *s: torch.randn(*s, dtype=torch.bfloat16) * 0.05)(B, S, D)),
            "wqkv": _w((lambda *s: torch.randn(*s, dtype=torch.bfloat16) * 0.05)(D, 3 * D)),
            "wo": _w((lambda *s: torch.randn(*s, dtype=torch.bfloat16) * 0.05)(D, D)),
        }
    _A_in = _A_setup()

    def _B_setup():
        B, S, D, F = 1, 2048, 512, 1792
        return {
            "x": _w((lambda *s: torch.randn(*s, dtype=torch.bfloat16) * 0.05)(B, S, D)),
            "w1": _w((lambda *s: torch.randn(*s, dtype=torch.bfloat16) * 0.8)(D, F) * (D ** -0.5)),
            "w2": _w((lambda *s: torch.randn(*s, dtype=torch.bfloat16) * 0.8)(F, D) * (F ** -0.5)),
        }
    _B_in = _B_setup()

    return {
        "A_x": _A_in["x"],
        "A_wqkv": _A_in["wqkv"],
        "A_wo": _A_in["wo"],
        "B_x": _B_in["x"],
        "B_w1": _B_in["w1"],
        "B_w2": _B_in["w2"],
    }

def reference(A_x, A_wqkv, A_wo, B_x, B_w1, B_w2):
    def _A_ref(x, wqkv, wo):
        B, S, D = x.shape
        H = 32; Dh = D // H
        qkv = (lambda x, *d: x.permute(*d))((x @ wqkv).reshape(B, S, 3, H, Dh), 2, 0, 3, 1, 4)
        q, k, v = qkv[0], qkv[1], qkv[2]
        phi = lambda t: torch.where(t > 0, t + 1, torch.exp(t)) + 1e-6
        pq, pk = phi(q), phi(k)
        kv = torch.einsum("bhsd,bhse->bhde", pk, v)
        z = torch.einsum("bhsd->bhd", pk)
        num = torch.einsum("bhqd,bhde->bhqe", pq, kv)
        den = torch.einsum("bhqd,bhd->bhq", pq, z)[..., None] + 1e-6
        o = num / den
        return (lambda x, *d: x.permute(*d))(o, 0, 2, 1, 3).reshape(B, S, D) @ wo

    def _B_ref(x, w1, w2):
        h = torch.einsum("bsd,df->bsf", x, w1)
        h = _silu(h)
        out = torch.einsum("bsf,fd->bsd", h, w2)
        return out

    x = A_x
    x = x + _A_ref(_layernorm(x, axis=-1), A_wqkv, A_wo)
    x = x + _B_ref(_layernorm(x, axis=-1), B_w1, B_w2)
    x = x + _A_ref(_layernorm(x, axis=-1), A_wqkv, A_wo)
    x = x + _B_ref(_layernorm(x, axis=-1), B_w1, B_w2)
    return x
```

```cpp
#include <hip/hip_runtime.h>
#include <hip/hip_bf16.h>
#include <stdint.h>

#define GAS __attribute__((address_space(1)))
#define LAS __attribute__((address_space(3)))
typedef unsigned v4u __attribute__((ext_vector_type(4)));
typedef unsigned v2u __attribute__((ext_vector_type(2)));
typedef float f32x4 __attribute__((ext_vector_type(4)));
typedef float f32x2 __attribute__((ext_vector_type(2)));
typedef __bf16 bf16x8 __attribute__((ext_vector_type(8)));
typedef __bf16 bf16x2 __attribute__((ext_vector_type(2)));
typedef short s16x4 __attribute__((ext_vector_type(4)));

namespace {
constexpr int S = 2048, D = 512, F = 1792, H = 32;
constexpr int NG = 32;
constexpr size_t KB = 1024;
constexpr size_t OFF_WQKV = 0;
constexpr size_t OFF_WO = OFF_WQKV + 96 * 16 * KB;
constexpr size_t OFF_W1 = OFF_WO + 32 * 16 * KB;
constexpr size_t OFF_W2 = OFF_W1 + 112 * 16 * KB;
constexpr size_t OFF_CSQ = OFF_W2 + 32 * 56 * KB;
constexpr size_t OFF_CS1 = OFF_CSQ + 8 * KB;
constexpr size_t XBYTES = (size_t)NG * 64 * KB;
constexpr size_t STBYTES = (size_t)NG * 8 * KB;
constexpr size_t OFF_X = OFF_CS1 + 8 * KB;
constexpr size_t OFF_ST = OFF_X + 4 * XBYTES;
constexpr size_t OFF_PQ = OFF_ST + 4 * STBYTES;
constexpr size_t OFF_KVP = OFF_PQ + 2 * XBYTES;
constexpr size_t KVPBYTES = (size_t)NG * H * KB;
constexpr size_t OFF_ZP = OFF_KVP + 2 * KVPBYTES;
constexpr size_t ZPBYTES = (size_t)NG * H * 64;
constexpr size_t OFF_KVR = OFF_ZP + 2 * ZPBYTES;
constexpr size_t KVRBYTES = (size_t)H * KB;
constexpr size_t OFF_H = OFF_KVR + 2 * KVRBYTES;
constexpr size_t HBYTES = (size_t)NG * 224 * KB;
constexpr size_t WS_TOTAL = OFF_H + 2 * HBYTES;

__device__ __forceinline__ f32x4 mfma32(v4u a, v4u b, f32x4 c) {
  return __builtin_amdgcn_mfma_f32_16x16x32_bf16(__builtin_bit_cast(bf16x8, a), __builtin_bit_cast(bf16x8, b), c, 0, 0, 0);
}
__device__ __forceinline__ f32x4 mfma16(v2u a, v2u b, f32x4 c) {
  return __builtin_amdgcn_mfma_f32_16x16x16bf16_1k(__builtin_bit_cast(s16x4, a), __builtin_bit_cast(s16x4, b), c, 0, 0, 0);
}
__device__ __forceinline__ unsigned pk2(float lo, float hi) {
  bf16x2 v; v.x = (__bf16)lo; v.y = (__bf16)hi;
  return __builtin_bit_cast(unsigned, v);
}
__device__ __forceinline__ float bflo(unsigned u) { return __builtin_bit_cast(float, u << 16); }
__device__ __forceinline__ float bfhi(unsigned u) { return __builtin_bit_cast(float, u & 0xFFFF0000u); }
__device__ __forceinline__ float phi(float t) { return (t > 0.f ? t + 1.f : __expf(t)) + 1e-6f; }
__device__ __forceinline__ int permk(int h, int i) { return i < 4 ? 4 * h + i : 16 + 4 * h + (i - 4); }
__device__ __forceinline__ void blk_gc(int b, int& g, int& c) { const int xcd = b & 7, idx = b >> 3; g = xcd * 4 + (idx >> 3); c = idx & 7; }

__global__ __launch_bounds__(256) void k_prep(const uint16_t* __restrict__ x, const uint16_t* __restrict__ wqkv, const uint16_t* __restrict__ wo,
                                              const uint16_t* __restrict__ w1, const uint16_t* __restrict__ w2, unsigned char* __restrict__ ws) {
  __shared__ __attribute__((aligned(16))) uint16_t sm[512 * 40];
  const int b = blockIdx.x, tid = threadIdx.x;
  if (b < 184) {
    const uint16_t* W; int N, K0, KN, nb; size_t off; int UT; float* cs = nullptr;
    if (b < 48) { W = wqkv; N = 1536; K0 = 0; KN = 512; nb = b; off = OFF_WQKV; UT = 16; cs = (float*)(ws + OFF_CSQ); }
    else if (b < 64) { W = wo; N = 512; K0 = 0; KN = 512; nb = b - 48; off = OFF_WO; UT = 16; }
    else if (b < 120) { W = w1; N = 1792; K0 = 0; KN = 512; nb = b - 64; off = OFF_W1; UT = 16; cs = (float*)(ws + OFF_CS1); }
    else { W = w2; N = 512; nb = (b - 120) >> 2; K0 = ((b - 120) & 3) * 448; KN = 448; off = OFF_W2; UT = 56; }
    for (int r = tid >> 2; r < KN; r += 64) {
      v4u v = *(const v4u*)(W + (size_t)(K0 + r) * N + nb * 32 + (tid & 3) * 8);
      *(v4u*)(sm + r * 40 + (tid & 3) * 8) = v;
    }
    __syncthreads();
    const int nu = KN / 32;
    for (int idx = tid; idx < 2 * nu * 64; idx += 256) {
      const int lane = idx & 63, u = (idx >> 6) % nu, t = (idx >> 6) / nu;
      const int j = lane & 15, h = lane >> 4;
      unsigned o[4];
#pragma unroll
      for (int i = 0; i < 8; i += 2) {
        unsigned lo = sm[(32 * u + permk(h, i)) * 40 + 16 * t + j], hi = sm[(32 * u + permk(h, i + 1)) * 40 + 16 * t + j];
        o[i >> 1] = lo | (hi << 16);
      }
      *(v4u*)(ws + off + ((size_t)((nb * 2 + t) * UT + (K0 / 32) + u) * 64 + lane) * 16) = v4u{o[0], o[1], o[2], o[3]};
    }
    if (cs) {
      const int col = tid >> 3, part = tid & 7;
      float s = 0.f;
      for (int r = part; r < 512; r += 8) s += bflo(sm[r * 40 + col]);
      s += __shfl_xor(s, 1); s += __shfl_xor(s, 2); s += __shfl_xor(s, 4);
      if (part == 0) cs[nb * 32 + col] = s;
    }
  } else {
    const int xb = b - 184, g = xb >> 2, cq = xb & 3;
    for (int r = tid >> 4; r < 64; r += 16) {
      v4u v = *(const v4u*)(x + (size_t)(g * 64 + r) * D + cq * 128 + (tid & 15) * 8);
      *(v4u*)(sm + r * 136 + (tid & 15) * 8) = v;
    }
    __syncthreads();
    for (int idx = tid; idx < 4 * 4 * 64; idx += 256) {
      const int lane = idx & 63, m = (idx >> 6) & 3, ul = idx >> 8;
      const int j = lane & 15, h = lane >> 4;
      const uint16_t* row = sm + (16 * m + j) * 136 + 32 * ul;
      v2u a = *(const v2u*)(row + 4 * h), bb = *(const v2u*)(row + 16 + 4 * h);
      *(v4u*)(ws + OFF_X + ((size_t)(g * 16 + cq * 4 + ul) * 4 + m) * KB + lane * 16) = v4u{a.x, a.y, bb.x, bb.y};
    }
    {
      const int tok = tid >> 2, pl = tid & 3;
      const uint16_t* row = sm + tok * 136 + 32 * pl;
      float s = 0.f;
      for (int i = 0; i < 32; ++i) s += bflo(row[i]);
      const float mean = s * (1.f / 32.f);
      float m2 = 0.f;
      for (int i = 0; i < 32; ++i) { float d = bflo(row[i]) - mean; m2 += d * d; }
      *(f32x2*)(ws + OFF_ST + (size_t)g * 8 * KB + (tok * 16 + cq * 4 + pl) * 8) = f32x2{mean, m2};
    }
  }
}

__device__ __forceinline__ void stage_x(const unsigned char* __restrict__ xg, const unsigned char* __restrict__ stg, LAS unsigned char* L, int tid) {
  const int lane = tid & 63, w = __builtin_amdgcn_readfirstlane(tid >> 6);
#pragma unroll
  for (int i = 0; i < 8; ++i)
    __builtin_amdgcn_global_load_lds((const GAS void*)(xg + (w * 8 + i) * KB + lane * 16), (LAS void*)(L + (w * 8 + i) * KB), 16, 0, 0);
  const int tok = tid >> 3, part = tid & 7;
  f32x4 s = *(const f32x4*)(stg + (tok * 16 + part * 2) * 8);
  float msum = s.x + s.z;
  msum += __shfl_xor(msum, 1); msum += __shfl_xor(msum, 2); msum += __shfl_xor(msum, 4);
  const float mean = msum * (1.f / 16.f);
  float m2 = s.y + s.w + 32.f * ((s.x - mean) * (s.x - mean) + (s.z - mean) * (s.z - mean));
  m2 += __shfl_xor(m2, 1); m2 += __shfl_xor(m2, 2); m2 += __shfl_xor(m2, 4);
  const float rstd = 1.0f / sqrtf(m2 * (1.f / 512.f) + 1e-5f);
  if (part == 0) {
    ((LAS float*)(L + 64 * KB))[tok] = mean;
    ((LAS float*)(L + 64 * KB + 256))[tok] = rstd;
  }
  asm volatile("s_waitcnt vmcnt(0)" ::: "memory");
  __syncthreads();
}

__global__ __launch_bounds__(512, 2) void k_qkv(unsigned char* __restrict__ ws, int xi, int layer) {
  extern __shared__ __attribute__((aligned(16))) unsigned char smem[];
  LAS unsigned char* L = (LAS unsigned char*)smem;
  const int tid = threadIdx.x, lane = tid & 63, w = __builtin_amdgcn_readfirstlane(tid >> 6);
  int g, c; blk_gc(blockIdx.x, g, c);
  const int j = lane & 15, h = lane >> 4;
  const float* cs = (const float*)(ws + OFF_CSQ);
  const int hd = 4 * c + (w & 3);
  const LAS v4u* xl = (const LAS v4u*)L + lane;
  const LAS float* meanL = (const LAS float*)(L + 64 * KB);
  const LAS float* rstdL = (const LAS float*)(L + 64 * KB + 256);
  if (w < 4) {
    const v4u* wk = (const v4u*)(ws + OFF_WQKV + (size_t)(32 + hd) * 16 * KB) + lane;
    const v4u* wv = (const v4u*)(ws + OFF_WQKV + (size_t)(64 + hd) * 16 * KB) + lane;
    v4u fk[16], fv[16];
#pragma unroll
    for (int u = 0; u < 16; ++u) { fk[u] = wk[u * 64]; fv[u] = wv[u * 64]; }
    __builtin_amdgcn_sched_barrier(0);
    stage_x(ws + OFF_X + xi * XBYTES + (size_t)g * 64 * KB, ws + OFF_ST + xi * STBYTES + (size_t)g * 8 * KB, L, tid);
    f32x4 ak[4], av[4];
#pragma unroll
    for (int m = 0; m < 4; ++m) { ak[m] = f32x4{0, 0, 0, 0}; av[m] = f32x4{0, 0, 0, 0}; }
#pragma unroll
    for (int u = 0; u < 16; ++u)
#pragma unroll
      for (int m = 0; m < 4; ++m) {
        v4u xf = xl[(u * 4 + m) * 64];
        ak[m] = mfma32(xf, fk[u], ak[m]);
        av[m] = mfma32(xf, fv[u], av[m]);
      }
    const float csk = cs[512 + 16 * hd + j], csv = cs[1024 + 16 * hd + j];
    unsigned pk[8], vv[8];
#pragma unroll
    for (int m = 0; m < 4; ++m) {
      f32x4 mean = *(const LAS f32x4*)(meanL + 16 * m + 4 * h), rstd = *(const LAS f32x4*)(rstdL + 16 * m + 4 * h);
      float kk[4], vq[4];
#pragma unroll
      for (int r = 0; r < 4; ++r) { kk[r] = phi((ak[m][r] - mean[r] * csk) * rstd[r]); vq[r] = (av[m][r] - mean[r] * csv) * rstd[r]; }
      pk[2 * m] = pk2(kk[0], kk[1]); pk[2 * m + 1] = pk2(kk[2], kk[3]);
      vv[2 * m] = pk2(vq[0], vq[1]); vv[2 * m + 1] = pk2(vq[2], vq[3]);
    }
    f32x4 kv = {0, 0, 0, 0}, z = {0, 0, 0, 0};
    const v4u ones = {0x3F803F80u, 0x3F803F80u, 0x3F803F80u, 0x3F803F80u};
#pragma unroll
    for (int mp = 0; mp < 2; ++mp) {
      v4u a = {pk[4 * mp], pk[4 * mp + 1], pk[4 * mp + 2], pk[4 * mp + 3]};
      v4u bq = {vv[4 * mp], vv[4 * mp + 1], vv[4 * mp + 2], vv[4 * mp + 3]};
      kv = mfma32(a, bq, kv);
      z = mfma32(a, ones, z);
    }
    *(f32x4*)(ws + OFF_KVP + layer * KVPBYTES + ((size_t)(g * H + hd) * 64 + lane) * 16) = kv;
    if (j == 0) *(f32x4*)(ws + OFF_ZP + layer * ZPBYTES + (size_t)(g * H + hd) * 64 + h * 16) = z;
  } else {
    const v4u* wq = (const v4u*)(ws + OFF_WQKV + (size_t)hd * 16 * KB) + lane;
    v4u fq[16];
#pragma unroll
    for (int u = 0; u < 16; ++u) fq[u] = wq[u * 64];
    __builtin_amdgcn_sched_barrier(0);
    stage_x(ws + OFF_X + xi * XBYTES + (size_t)g * 64 * KB, ws + OFF_ST + xi * STBYTES + (size_t)g * 8 * KB, L, tid);
    f32x4 aq[4];
#pragma unroll
    for (int m = 0; m < 4; ++m) aq[m] = f32x4{0, 0, 0, 0};
#pragma unroll
    for (int u = 0; u < 16; ++u)
#pragma unroll
      for (int m = 0; m < 4; ++m) aq[m] = mfma32(fq[u], xl[(u * 4 + m) * 64], aq[m]);
    const f32x4 csq = *(const f32x4*)(cs + 16 * hd + 4 * h);
    unsigned pq[8];
#pragma unroll
    for (int m = 0; m < 4; ++m) {
      const float mean = meanL[16 * m + j], rstd = rstdL[16 * m + j];
      float q[4];
#pragma unroll
      for (int r = 0; r < 4; ++r) q[r] = phi((aq[m][r] - mean * csq[r]) * rstd);
      pq[2 * m] = pk2(q[0], q[1]); pq[2 * m + 1] = pk2(q[2], q[3]);
    }
#pragma unroll
    for (int mp = 0; mp < 2; ++mp)
      *(v4u*)(ws + OFF_PQ + layer * XBYTES + ((size_t)((g * H + hd) * 2 + mp) * 64 + lane) * 16) = v4u{pq[4 * mp], pq[4 * mp + 1], pq[4 * mp + 2], pq[4 * mp + 3]};
  }
}

__global__ __launch_bounds__(64) void k_red(unsigned char* __restrict__ ws, int layer) {
  const int hd = blockIdx.x, lane = threadIdx.x, h = lane >> 4;
  f32x4 s = {0, 0, 0, 0}, z = {0, 0, 0, 0};
#pragma unroll 8
  for (int g = 0; g < NG; ++g) {
    s += *(const f32x4*)(ws + OFF_KVP + layer * KVPBYTES + ((size_t)(g * H + hd) * 64 + lane) * 16);
    z += *(const f32x4*)(ws + OFF_ZP + layer * ZPBYTES + (size_t)(g * H + hd) * 64 + h * 16);
  }
  *(v2u*)(ws + OFF_KVR + layer * KVRBYTES + (size_t)hd * KB + lane * 8) = v2u{pk2(s.x, s.y), pk2(s.z, s.w)};
  *(v2u*)(ws + OFF_KVR + layer * KVRBYTES + (size_t)hd * KB + 512 + lane * 8) = v2u{pk2(z.x, z.y), pk2(z.z, z.w)};
}

template <bool FINAL>
__device__ __forceinline__ void reduce_out(f32x4 (&acc)[4][4], LAS unsigned char* L, const unsigned char* xres_g, unsigned char* xout_g, unsigned char* stout_g,
                                           uint16_t* out, int g, int c, int tid) {
  const int lane = tid & 63, w = __builtin_amdgcn_readfirstlane(tid >> 6);
  LAS f32x4* red = (LAS f32x4*)L;
#pragma unroll
  for (int nt = 0; nt < 4; ++nt)
#pragma unroll
    for (int m = 0; m < 4; ++m) red[((w * 4 + nt) * 4 + m) * 64 + lane] = acc[nt][m];
  const int m = w >> 1, uu = w & 1;
  const v4u res = *(const v4u*)(xres_g + ((size_t)(2 * c + uu) * 4 + m) * KB + lane * 16);
  __syncthreads();
  f32x4 s0 = {0, 0, 0, 0}, s1 = {0, 0, 0, 0};
#pragma unroll
  for (int ww = 0; ww < 8; ++ww) {
    s0 += red[((ww * 4 + 2 * uu) * 4 + m) * 64 + lane];
    s1 += red[((ww * 4 + 2 * uu + 1) * 4 + m) * 64 + lane];
  }
  s0.x += bflo(res.x); s0.y += bfhi(res.x); s0.z += bflo(res.y); s0.w += bfhi(res.y);
  s1.x += bflo(res.z); s1.y += bfhi(res.z); s1.z += bflo(res.w); s1.w += bfhi(res.w);
  v4u o = {pk2(s0.x, s0.y), pk2(s0.z, s0.w), pk2(s1.x, s1.y), pk2(s1.z, s1.w)};
  const int j = lane & 15, h = lane >> 4;
  if constexpr (FINAL) {
    uint16_t* row = out + (size_t)(g * 64 + 16 * m + j) * D + 64 * c + 32 * uu;
    *(v2u*)(row + 4 * h) = v2u{o.x, o.y};
    *(v2u*)(row + 16 + 4 * h) = v2u{o.z, o.w};
  } else {
    *(v4u*)(xout_g + ((size_t)(2 * c + uu) * 4 + m) * KB + lane * 16) = o;
    float v[8] = {bflo(o.x), bfhi(o.x), bflo(o.y), bfhi(o.y), bflo(o.z), bfhi(o.z), bflo(o.w), bfhi(o.w)};
    float s = ((v[0] + v[1]) + (v[2] + v[3])) + ((v[4] + v[5]) + (v[6] + v[7]));
    s += __shfl_xor(s, 16); s += __shfl_xor(s, 32);
    const float mean = s * (1.f / 32.f);
    float m2 = 0.f;
#pragma unroll
    for (int i = 0; i < 8; ++i) { float d = v[i] - mean; m2 += d * d; }
    m2 += __shfl_xor(m2, 16); m2 += __shfl_xor(m2, 32);
    if (h == 0) *(f32x2*)(stout_g + ((16 * m + j) * 16 + 2 * c + uu) * 8) = f32x2{mean, m2};
  }
}

__global__ __launch_bounds__(512, 2) void k_wo(unsigned char* __restrict__ ws, int xi, int layer) {
  extern __shared__ __attribute__((aligned(16))) unsigned char smem[];
  LAS unsigned char* L = (LAS unsigned char*)smem;
  const int tid = threadIdx.x, lane = tid & 63, w = __builtin_amdgcn_readfirstlane(tid >> 6);
  int g, c; blk_gc(blockIdx.x, g, c);
  v4u wf[4][2];
#pragma unroll
  for (int nt = 0; nt < 4; ++nt)
#pragma unroll
    for (int uu = 0; uu < 2; ++uu) wf[nt][uu] = *(const v4u*)(ws + OFF_WO + ((size_t)((4 * c + nt) * 16 + 2 * w + uu) * 64 + lane) * 16);
  v4u pq[4][2];
  v2u kvf[4], zf[4];
#pragma unroll
  for (int hh = 0; hh < 4; ++hh) {
    const int hd = 4 * w + hh;
#pragma unroll
    for (int mp = 0; mp < 2; ++mp) pq[hh][mp] = *(const v4u*)(ws + OFF_PQ + layer * XBYTES + ((size_t)((g * H + hd) * 2 + mp) * 64 + lane) * 16);
    kvf[hh] = *(const v2u*)(ws + OFF_KVR + layer * KVRBYTES + (size_t)hd * KB + lane * 8);
    zf[hh] = *(const v2u*)(ws + OFF_KVR + layer * KVRBYTES + (size_t)hd * KB + 512 + lane * 8);
  }
  v4u of[2][4];
  const f32x4 zero = {0, 0, 0, 0};
#pragma unroll
  for (int hh = 0; hh < 4; ++hh)
#pragma unroll
    for (int m = 0; m < 4; ++m) {
      const v4u p = pq[hh][m >> 1];
      const v2u b = (m & 1) ? v2u{p.z, p.w} : v2u{p.x, p.y};
      f32x4 num = mfma16(kvf[hh], b, zero);
      f32x4 den = mfma16(zf[hh], b, zero);
      float o0 = num.x / (den.x + 1e-6f), o1 = num.y / (den.y + 1e-6f), o2 = num.z / (den.z + 1e-6f), o3 = num.w / (den.w + 1e-6f);
      const unsigned lo = pk2(o0, o1), hi = pk2(o2, o3);
      if (hh & 1) { of[hh >> 1][m].z = lo; of[hh >> 1][m].w = hi; } else { of[hh >> 1][m].x = lo; of[hh >> 1][m].y = hi; }
    }
  f32x4 acc[4][4];
#pragma unroll
  for (int nt = 0; nt < 4; ++nt)
#pragma unroll
    for (int m = 0; m < 4; ++m) {
      acc[nt][m] = mfma32(wf[nt][0], of[0][m], zero);
      acc[nt][m] = mfma32(wf[nt][1], of[1][m], acc[nt][m]);
    }
  reduce_out<false>(acc, L, ws + OFF_X + xi * XBYTES + (size_t)g * 64 * KB, ws + OFF_X + (xi + 1) * XBYTES + (size_t)g * 64 * KB,
                    ws + OFF_ST + (xi + 1) * STBYTES + (size_t)g * 8 * KB, nullptr, g, c, tid);
}

__global__ __launch_bounds__(512, 2) void k_ffn1(unsigned char* __restrict__ ws, int xi, int layer) {
  extern __shared__ __attribute__((aligned(16))) unsigned char smem[];
  LAS unsigned char* L = (LAS unsigned char*)smem;
  const int tid = threadIdx.x, lane = tid & 63, w = __builtin_amdgcn_readfirstlane(tid >> 6);
  int g, c; blk_gc(blockIdx.x, g, c);
  const int j = lane & 15, h = lane >> 4;
  const LAS v4u* xl = (const LAS v4u*)L + lane;
  const LAS float* meanL = (const LAS float*)(L + 64 * KB);
  const LAS float* rstdL = (const LAS float*)(L + 64 * KB + 256);
  const int wq = w < 7 ? w : 6;
  const int nt0 = 14 * c + 2 * wq;
  const v4u* w0 = (const v4u*)(ws + OFF_W1 + (size_t)nt0 * 16 * KB) + lane;
  v4u f0[16], f1[16];
  if (w < 7) {
#pragma unroll
    for (int u = 0; u < 16; ++u) { f0[u] = w0[u * 64]; f1[u] = w0[(16 + u) * 64]; }
  }
  __builtin_amdgcn_sched_barrier(0);
  stage_x(ws + OFF_X + xi * XBYTES + (size_t)g * 64 * KB, ws + OFF_ST + xi * STBYTES + (size_t)g * 8 * KB, L, tid);
  if (w < 7) {
    f32x4 a0[4], a1[4];
#pragma unroll
    for (int m = 0; m < 4; ++m) { a0[m] = f32x4{0, 0, 0, 0}; a1[m] = f32x4{0, 0, 0, 0}; }
#pragma unroll
    for (int u = 0; u < 16; ++u)
#pragma unroll
      for (int m = 0; m < 4; ++m) {
        v4u xf = xl[(u * 4 + m) * 64];
        a0[m] = mfma32(f0[u], xf, a0[m]);
        a1[m] = mfma32(f1[u], xf, a1[m]);
      }
    const float* cs = (const float*)(ws + OFF_CS1);
    const f32x4 c0 = *(const f32x4*)(cs + 16 * nt0 + 4 * h), c1 = *(const f32x4*)(cs + 16 * nt0 + 16 + 4 * h);
#pragma unroll
    for (int m = 0; m < 4; ++m) {
      const float mean = meanL[16 * m + j], rstd = rstdL[16 * m + j];
      float t[8];
#pragma unroll
      for (int r = 0; r < 4; ++r) { t[r] = (a0[m][r] - mean * c0[r]) * rstd; t[4 + r] = (a1[m][r] - mean * c1[r]) * rstd; }
#pragma unroll
      for (int r = 0; r < 8; ++r) t[r] = t[r] / (1.f + __expf(-t[r]));
      *(v4u*)(ws + OFF_H + layer * HBYTES + ((size_t)(g * 56 + 7 * c + w) * 4 + m) * KB + lane * 16) =
          v4u{pk2(t[0], t[1]), pk2(t[2], t[3]), pk2(t[4], t[5]), pk2(t[6], t[7])};
    }
  }
}

template <bool FINAL>
__global__ __launch_bounds__(512, 2) void k_ffn2(unsigned char* __restrict__ ws, int xi, int layer, uint16_t* __restrict__ out) {
  extern __shared__ __attribute__((aligned(16))) unsigned char smem[];
  LAS unsigned char* L = (LAS unsigned char*)smem;
  const int tid = threadIdx.x, lane = tid & 63, w = __builtin_amdgcn_readfirstlane(tid >> 6);
  int g, c; blk_gc(blockIdx.x, g, c);
  f32x4 acc[4][4];
#pragma unroll
  for (int nt = 0; nt < 4; ++nt)
#pragma unroll
    for (int m = 0; m < 4; ++m) acc[nt][m] = f32x4{0, 0, 0, 0};
  const v4u* wp = (const v4u*)(ws + OFF_W2 + ((size_t)(4 * c) * 56 + 7 * w) * KB) + lane;
  const v4u* hp = (const v4u*)(ws + OFF_H + layer * HBYTES + ((size_t)(g * 56 + 7 * w) * 4) * KB) + lane;
  v4u wf[7][4];
#pragma unroll
  for (int u = 0; u < 7; ++u)
#pragma unroll
    for (int nt = 0; nt < 4; ++nt) wf[u][nt] = wp[(nt * 56 + u) * 64];
#pragma unroll
  for (int u = 0; u < 7; ++u) {
    v4u hf[4];
#pragma unroll
    for (int m = 0; m < 4; ++m) hf[m] = hp[(u * 4 + m) * 64];
#pragma unroll
    for (int nt = 0; nt < 4; ++nt)
#pragma unroll
      for (int m = 0; m < 4; ++m) acc[nt][m] = mfma32(wf[u][nt], hf[m], acc[nt][m]);
  }
  reduce_out<FINAL>(acc, L, ws + OFF_X + xi * XBYTES + (size_t)g * 64 * KB, ws + OFF_X + (xi + 1) * XBYTES + (size_t)g * 64 * KB,
                    ws + OFF_ST + (xi + 1) * STBYTES + (size_t)g * 8 * KB, out, g, c, tid);
}
}

extern "C" void kernel_launch(void* const* d_in, const int* in_sizes, int n_in, void* d_out, int out_size, void* d_ws, size_t ws_size, hipStream_t stream) {
  const uint16_t* x = (const uint16_t*)d_in[0];
  const uint16_t* wqkv = (const uint16_t*)d_in[1];
  const uint16_t* wo = (const uint16_t*)d_in[2];
  const uint16_t* w1 = (const uint16_t*)d_in[4];
  const uint16_t* w2 = (const uint16_t*)d_in[5];
  unsigned char* ws = (unsigned char*)d_ws;
  uint16_t* out = (uint16_t*)d_out;
  constexpr int LDS1 = 64 * 1024 + 512, LDS2 = 128 * 1024;
  (void)hipFuncSetAttribute((const void*)k_qkv, hipFuncAttributeMaxDynamicSharedMemorySize, LDS1);
  (void)hipFuncSetAttribute((const void*)k_ffn1, hipFuncAttributeMaxDynamicSharedMemorySize, LDS1);
  (void)hipFuncSetAttribute((const void*)k_wo, hipFuncAttributeMaxDynamicSharedMemorySize, LDS2);
  (void)hipFuncSetAttribute((const void*)k_ffn2<false>, hipFuncAttributeMaxDynamicSharedMemorySize, LDS2);
  (void)hipFuncSetAttribute((const void*)k_ffn2<true>, hipFuncAttributeMaxDynamicSharedMemorySize, LDS2);
  k_prep<<<312, 256, 0, stream>>>(x, wqkv, wo, w1, w2, ws);
  for (int l = 0; l < 2; ++l) {
    const int xi = 2 * l;
    k_qkv<<<256, 512, LDS1, stream>>>(ws, xi, l);
    k_red<<<32, 64, 0, stream>>>(ws, l);
    k_wo<<<256, 512, LDS2, stream>>>(ws, xi, l);
    k_ffn1<<<256, 512, LDS1, stream>>>(ws, xi + 1, l);
    if (l == 0) k_ffn2<false><<<256, 512, LDS2, stream>>>(ws, xi + 1, l, out);
    else k_ffn2<true><<<256, 512, LDS2, stream>>>(ws, xi + 1, l, out);
  }
}
```

```cpp
#include <hip/hip_runtime.h>
#include <hip/hip_bf16.h>
#include <stdint.h>

#define GAS __attribute__((address_space(1)))
#define LAS __attribute__((address_space(3)))
typedef unsigned v4u __attribute__((ext_vector_type(4)));
typedef unsigned v2u __attribute__((ext_vector_type(2)));
typedef float f32x4 __attribute__((ext_vector_type(4)));
typedef float f32x2 __attribute__((ext_vector_type(2)));
typedef __bf16 bf16x8 __attribute__((ext_vector_type(8)));
typedef __bf16 bf16x2 __attribute__((ext_vector_type(2)));
typedef short s16x4 __attribute__((ext_vector_type(4)));

#define STAMP(slot)
#define STAMP_DECL
#define STAMP_FLUSH

namespace {
constexpr int S = 2048, D = 512, F = 1792, H = 32;
constexpr int NG = 32;
constexpr int KB = 1024;
constexpr unsigned SENT = 0xFFFFFFFFu;
constexpr int OFF_WQKV = 0;
constexpr int OFF_WO = OFF_WQKV + 96 * 16 * KB;
constexpr int OFF_W1 = OFF_WO + 32 * 16 * KB;
constexpr int OFF_W2 = OFF_W1 + 112 * 16 * KB;
constexpr int OFF_CSQ = OFF_W2 + 32 * 56 * KB;
constexpr int OFF_CS1 = OFF_CSQ + 8 * KB;
constexpr int OFF_TMO = OFF_CS1 + 8 * KB;
constexpr int XBYTES = NG * 64 * KB;
constexpr int STBYTES = NG * 8 * KB;
constexpr int OFF_X0 = OFF_TMO + 4 * KB;
constexpr int OFF_ST0 = OFF_X0 + XBYTES;
constexpr int OFF_FILL0 = OFF_ST0 + STBYTES;
constexpr int OFF_X1 = OFF_FILL0;
constexpr int OFF_ST1 = OFF_X1 + 3 * XBYTES;
constexpr int OFF_PQ = OFF_ST1 + 3 * STBYTES;
constexpr int KVPBYTES = NG * H * KB;
constexpr int OFF_KVP = OFF_PQ + 2 * XBYTES;
constexpr int ZPBYTES = NG * H * 64;
constexpr int OFF_ZP = OFF_KVP + 2 * KVPBYTES;
constexpr int KVRBYTES = 16 * KB;
constexpr int OFF_KVR = OFF_ZP + 2 * ZPBYTES;
constexpr int ZRBYTES = KB;
constexpr int OFF_ZR = OFF_KVR + 2 * KVRBYTES;
constexpr int HBYTES = NG * 224 * KB;
constexpr int OFF_H = OFF_ZR + 2 * ZRBYTES;
constexpr int OFF_FILL1 = OFF_H + 2 * HBYTES;
constexpr int WS_TOTAL = OFF_FILL1;
static_assert((OFF_FILL0 % 1024) == 0 && (OFF_FILL1 % 1024) == 0, "");
__host__ __device__ constexpr int xoff(int xi) { return xi == 0 ? OFF_X0 : OFF_X1 + (xi - 1) * XBYTES; }
__host__ __device__ constexpr int stoff(int xi) { return xi == 0 ? OFF_ST0 : OFF_ST1 + (xi - 1) * STBYTES; }

__device__ __forceinline__ f32x4 mfma32(v4u a, v4u b, f32x4 c) {
  return __builtin_amdgcn_mfma_f32_16x16x32_bf16(__builtin_bit_cast(bf16x8, a), __builtin_bit_cast(bf16x8, b), c, 0, 0, 0);
}
__device__ __forceinline__ f32x4 mfma16(v2u a, v2u b, f32x4 c) {
  return __builtin_amdgcn_mfma_f32_16x16x16bf16_1k(__builtin_bit_cast(s16x4, a), __builtin_bit_cast(s16x4, b), c, 0, 0, 0);
}
__device__ __forceinline__ unsigned pk2(float lo, float hi) {
  bf16x2 v; v.x = (__bf16)lo; v.y = (__bf16)hi;
  return __builtin_bit_cast(unsigned, v);
}
__device__ __forceinline__ unsigned san(unsigned u) { return u < 0xFFFFFFFEu ? u : 0xFFFFFFFEu; }
__device__ __forceinline__ unsigned pk2s(float lo, float hi) { return san(pk2(lo, hi)); }
__device__ __forceinline__ float asf(unsigned u) { return __builtin_bit_cast(float, u); }
__device__ __forceinline__ float bflo(unsigned u) { return __builtin_bit_cast(float, u << 16); }
__device__ __forceinline__ float bfhi(unsigned u) { return __builtin_bit_cast(float, u & 0xFFFF0000u); }
__device__ __forceinline__ float fexp(float t) { return __builtin_amdgcn_exp2f(t * 1.44269504088896f); }
__device__ __forceinline__ float phi(float t) { return fmaxf(t, 0.f) + fexp(fminf(t, 0.f)) + 1e-6f; }
__device__ __forceinline__ int permk(int h, int i) { return i < 4 ? 4 * h + i : 16 + 4 * h + (i - 4); }
__device__ __forceinline__ void blk_gc(int b, int& g, int& c) { const int xcd = b & 7, idx = b >> 3; g = xcd * 4 + (idx >> 3); c = idx & 7; }
__device__ __forceinline__ unsigned umax(unsigned a, unsigned b) { return a > b ? a : b; }
__device__ __forceinline__ unsigned umax4(v4u v) { return umax(umax(v.x, v.y), umax(v.z, v.w)); }
#define SB() __builtin_amdgcn_sched_barrier(0)

__global__ __launch_bounds__(256) void k_prep(const uint16_t* __restrict__ x, const uint16_t* __restrict__ wqkv, const uint16_t* __restrict__ wo,
                                              const uint16_t* __restrict__ w1, const uint16_t* __restrict__ w2, unsigned char* __restrict__ ws) {
  __shared__ __attribute__((aligned(16))) uint16_t sm[512 * 40];
  const int b = blockIdx.x, tid = threadIdx.x;
  {
    const int n16 = (OFF_FILL1 - OFF_FILL0) / 16;
    v4u* p = (v4u*)(ws + OFF_FILL0);
    for (int i = b * 256 + tid; i < n16; i += gridDim.x * 256) p[i] = v4u{SENT, SENT, SENT, SENT};
    if (b == 0 && tid == 0) *(unsigned*)(ws + OFF_TMO) = 0u;
  }
  if (b < 184) {
    const uint16_t* W; int N, K0, KN, nb; size_t off; int UT; float* cs = nullptr;
    if (b < 48) { W = wqkv; N = 1536; K0 = 0; KN = 512; nb = b; off = OFF_WQKV; UT = 16; cs = (float*)(ws + OFF_CSQ); }
    else if (b < 64) { W = wo; N = 512; K0 = 0; KN = 512; nb = b - 48; off = OFF_WO; UT = 16; }
    else if (b < 120) { W = w1; N = 1792; K0 = 0; KN = 512; nb = b - 64; off = OFF_W1; UT = 16; cs = (float*)(ws + OFF_CS1); }
    else { W = w2; N = 512; nb = (b - 120) >> 2; K0 = ((b - 120) & 3) * 448; KN = 448; off = OFF_W2; UT = 56; }
    for (int r = tid >> 2; r < KN; r += 64) {
      v4u v = *(const v4u*)(W + (size_t)(K0 + r) * N + nb * 32 + (tid & 3) * 8);
      *(v4u*)(sm + r * 40 + (tid & 3) * 8) = v;
    }
    __syncthreads();
    const int nu = KN / 32;
    for (int idx = tid; idx < 2 * nu * 64; idx += 256) {
      const int lane = idx & 63, u = (idx >> 6) % nu, t = (idx >> 6) / nu;
      const int j = lane & 15, h = lane >> 4;
      unsigned o[4];
#pragma unroll
      for (int i = 0; i < 8; i += 2) {
        unsigned lo = sm[(32 * u + permk(h, i)) * 40 + 16 * t + j], hi = sm[(32 * u + permk(h, i + 1)) * 40 + 16 * t + j];
        o[i >> 1] = lo | (hi << 16);
      }
      *(v4u*)(ws + off + ((size_t)((nb * 2 + t) * UT + (K0 / 32) + u) * 64 + lane) * 16) = v4u{o[0], o[1], o[2], o[3]};
    }
    if (cs) {
      const int col = tid >> 3, part = tid & 7;
      float s = 0.f;
      for (int r = part; r < 512; r += 8) s += bflo(sm[r * 40 + col]);
      s += __shfl_xor(s, 1); s += __shfl_xor(s, 2); s += __shfl_xor(s, 4);
      if (part == 0) cs[nb * 32 + col] = s;
    }
  } else {
    const int xb = b - 184, g = xb >> 2, cq = xb & 3;
    for (int r = tid >> 4; r < 64; r += 16) {
      v4u v = *(const v4u*)(x + (size_t)(g * 64 + r) * D + cq * 128 + (tid & 15) * 8);
      *(v4u*)(sm + r * 136 + (tid & 15) * 8) = v;
    }
    __syncthreads();
    for (int idx = tid; idx < 4 * 4 * 64; idx += 256) {
      const int lane = idx & 63, m = (idx >> 6) & 3, ul = idx >> 8;
      const int j = lane & 15, h = lane >> 4;
      const uint16_t* row = sm + (16 * m + j) * 136 + 32 * ul;
      v2u a = *(const v2u*)(row + 4 * h), bb = *(const v2u*)(row + 16 + 4 * h);
      *(v4u*)(ws + OFF_X0 + (size_t)(((g * 2 + (m >> 1)) * 16 + cq * 4 + ul) * 2 + (m & 1)) * KB + lane * 16) = v4u{a.x, a.y, bb.x, bb.y};
    }
    {
      const int tok = tid >> 2, pl = tid & 3;
      const uint16_t* row = sm + tok * 136 + 32 * pl;
      float s = 0.f;
      for (int i = 0; i < 32; ++i) s += bflo(row[i]);
      const float mean = s * (1.f / 32.f);
      float m2 = 0.f;
      for (int i = 0; i < 32; ++i) { float d = bflo(row[i]) - mean; m2 += d * d; }
      *(f32x2*)(ws + OFF_ST0 + (size_t)(g * 2 + (tok >> 5)) * 4 * KB + ((tok & 31) * 16 + cq * 4 + pl) * 8) = f32x2{mean, m2};
    }
  }
}

typedef __amdgpu_buffer_rsrc_t rsrc_t;
constexpr int SC1 = 16;
__device__ __forceinline__ v4u ld128(rsrc_t rs, int voff, int soff) { return __builtin_amdgcn_raw_buffer_load_b128(rs, voff, soff, 0); }
__device__ __forceinline__ v4u ld128c(rsrc_t rs, int voff, int soff) { return __builtin_amdgcn_raw_buffer_load_b128(rs, voff, soff, SC1); }
__device__ __forceinline__ v2u ld64c(rsrc_t rs, int voff, int soff) { return __builtin_amdgcn_raw_buffer_load_b64(rs, voff, soff, SC1); }
__device__ __forceinline__ unsigned ld32c(rsrc_t rs, int voff, int soff) { return __builtin_amdgcn_raw_buffer_load_b32(rs, voff, soff, SC1); }
__device__ __forceinline__ void st128c(rsrc_t rs, v4u v, int voff, int soff) { __builtin_amdgcn_raw_buffer_store_b128(v, rs, (int)((unsigned)voff + (unsigned)soff), 0, SC1); }
__device__ __forceinline__ void st64c(rsrc_t rs, v2u v, int voff, int soff) { __builtin_amdgcn_raw_buffer_store_b64(v, rs, voff, soff, SC1); }
__device__ __forceinline__ void st32c(rsrc_t rs, unsigned v, int voff, int soff) { __builtin_amdgcn_raw_buffer_store_b32(v, rs, voff, soff, SC1); }
__device__ __forceinline__ bool valid(unsigned m) { return __builtin_amdgcn_ballot_w64(m == SENT) == 0ull; }
__device__ __forceinline__ unsigned asu(float f) { return __builtin_bit_cast(unsigned, f); }
template <int N> __device__ __forceinline__ float ror_add(float x) {
  return x + asf((unsigned)__builtin_amdgcn_update_dpp(0, (int)asu(x), 0x120 | N, 0xF, 0xF, false));
}
__device__ __forceinline__ float sum_x16(float x) { auto r = __builtin_amdgcn_permlane16_swap(asu(x), asu(x), false, false); return asf(r[0]) + asf(r[1]); }
__device__ __forceinline__ float sum_x32(float x) { auto r = __builtin_amdgcn_permlane32_swap(asu(x), asu(x), false, false); return asf(r[0]) + asf(r[1]); }
constexpr unsigned SPIN_CAP = 1u << 14;
__device__ __forceinline__ bool spin_fail(unsigned& n, rsrc_t rs) {
  asm volatile("" ::: "memory");
  __builtin_amdgcn_s_sleep(2);
  ++n;
  if (n > SPIN_CAP) { st32c(rs, 1u, OFF_TMO, 0); return true; }
  if ((n & 63u) == 0u) { if (__builtin_amdgcn_readfirstlane(ld32c(rs, OFF_TMO, 0)) != 0) return true; }
  return false;
}

#define H1_POS 1
#define W1_EARLY 14
#define KVR_SLEEP 4
#define W1_MID 6
#define W2_EARLY 8
constexpr int LDS_REG = 64 * KB;
constexpr int LDS_STAT = 2 * LDS_REG;
constexpr int LDS_CS = LDS_STAT + 512;
constexpr int LDS_RES = LDS_CS + 2048;
constexpr int LDS_VX = LDS_RES + 8 * KB;
constexpr int LDS_BYTES = LDS_VX + 8 * KB;
constexpr int OOBV = 0x7FFF0000;

__device__ __forceinline__ int fresh_lane() {
  int ln;
  asm volatile("v_mbcnt_lo_u32_b32 %0, -1, 0\n\tv_mbcnt_hi_u32_b32 %0, -1, %0" : "=v"(ln));
  return ln;
}
#define OPQ(x) asm volatile("" : "+s"(x))

__device__ __forceinline__ unsigned hmax(const v4u (&a)[14]) {
  unsigned m = 0;
#pragma unroll
  for (int i = 0; i < 14; ++i) m = umax(m, umax4(a[i]));
  return m;
}
struct XS { v4u x[4]; v2u s; };
__device__ __forceinline__ void x_issue(XS& a, rsrc_t rs, int xo, int so) {
  const int ln = fresh_lane();
#pragma unroll
  for (int i = 0; i < 4; ++i) a.x[i] = ld128c(rs, ln * 16, xo + i * KB);
  a.s = ld64c(rs, ln * 8, so);
}
__device__ __forceinline__ unsigned x_max(const XS& a) { return umax(umax(umax4(a.x[0]), umax4(a.x[1])), umax(umax(umax4(a.x[2]), umax4(a.x[3])), umax(a.s.x, a.s.y))); }
__device__ __forceinline__ void x_finish(XS& a, rsrc_t rs, int xo, int so, LAS unsigned char* L, int par, int w) {
  if (__builtin_expect(!valid(x_max(a)), 0)) {
    unsigned n = 0;
    do {
      if (spin_fail(n, rs)) break;
      x_issue(a, rs, xo, so);
    } while (!valid(x_max(a)));
  }
  const int ln = fresh_lane();
  LAS unsigned char* dst = L + par * LDS_REG + w * 4096 + ln * 16;
#pragma unroll
  for (int i = 0; i < 4; ++i) *(LAS v4u*)(dst + i * KB) = a.x[i];
  const float mp = asf(a.s.x), m2p = asf(a.s.y);
  const float mean = ror_add<1>(ror_add<2>(ror_add<4>(ror_add<8>(mp)))) * (1.f / 16.f);
  const float dm = mp - mean;
  const float m2 = ror_add<1>(ror_add<2>(ror_add<4>(ror_add<8>(m2p + 32.f * dm * dm))));
  const float rstd = __builtin_amdgcn_rsqf(m2 * (1.f / 512.f) + 1e-5f);
  if ((ln & 15) == 0) {
    LAS float* st = (LAS float*)(L + LDS_STAT + par * 256) + w * 4 + (ln >> 4);
    st[0] = mean; st[32] = rstd;
  }
  __syncthreads();
}

__device__ __forceinline__ void red_write(f32x4 (&acc)[4][2], LAS unsigned char* L, int par, int w) {
  const int ln = fresh_lane();
  LAS f32x4* red = (LAS f32x4*)(L + par * LDS_REG + w * 8 * KB + ln * 16);
#pragma unroll
  for (int nt = 0; nt < 4; ++nt)
#pragma unroll
    for (int mm = 0; mm < 2; ++mm) red[(nt * 2 + mm) * 64] = acc[nt][mm];
}
template <bool LAST>
__device__ __forceinline__ void red_out(rsrc_t rsw, rsrc_t rso, LAS unsigned char* L, int par, int xo, int so, int g, int c, int hf, int w) {
  const int ln = fresh_lane();
  const int mm = (w >> 1) & 1, uu = w & 1;
  const int j = ln & 15, h = ln >> 4;
  v4u o = {0, 0, 0, 0};
  v2u stv = {0, 0};
  if (w < 4) {
    const LAS f32x4* red = (const LAS f32x4*)(L + par * LDS_REG + (4 * uu + mm) * KB + ln * 16);
    LAS v4u* resp = (LAS v4u*)(L + LDS_RES + (hf * 4 + w) * KB + ln * 16);
    const v4u resid = *resp;
    f32x4 s0 = {0, 0, 0, 0}, s1 = {0, 0, 0, 0};
#pragma unroll
    for (int ww = 0; ww < 8; ++ww) {
      s0 += red[ww * 512];
      s1 += red[ww * 512 + 128];
    }
    s0.x += bflo(resid.x); s0.y += bfhi(resid.x); s0.z += bflo(resid.y); s0.w += bfhi(resid.y);
    s1.x += bflo(resid.z); s1.y += bfhi(resid.z); s1.z += bflo(resid.w); s1.w += bfhi(resid.w);
    o = v4u{pk2s(s0.x, s0.y), pk2s(s0.z, s0.w), pk2s(s1.x, s1.y), pk2s(s1.z, s1.w)};
    *resp = o;
  }
  asm volatile("" : "+v"(o));
  st128c(rsw, o, ln * 16, xo + ((2 * c + uu) * 2 + mm) * KB);
  if constexpr (LAST) {
    const int voff = j * (D * 2) + h * 8, soff = ((g * 64 + hf * 32 + 16 * mm) * D + 64 * c + 32 * uu) * 2;
    __builtin_amdgcn_raw_buffer_store_b64(v2u{o.x, o.y}, rso, voff, soff, 0);
    __builtin_amdgcn_raw_buffer_store_b64(v2u{o.z, o.w}, rso, voff, soff + 32, 0);
  }
  if (w < 4) {
    float v[8] = {bflo(o.x), bfhi(o.x), bflo(o.y), bfhi(o.y), bflo(o.z), bfhi(o.z), bflo(o.w), bfhi(o.w)};
    const float s = sum_x32(sum_x16(((v[0] + v[1]) + (v[2] + v[3])) + ((v[4] + v[5]) + (v[6] + v[7]))));
    const float mean = s * (1.f / 32.f);
    float m2 = 0.f;
#pragma unroll
    for (int i = 0; i < 8; ++i) { float d = v[i] - mean; m2 += d * d; }
    m2 = sum_x32(sum_x16(m2));
    stv = v2u{san(asu(mean)), san(asu(m2))};
  }
  asm volatile("" : "+v"(stv));
  st64c(rsw, stv, h == 0 ? j * 128 : OOBV, so + (16 * mm * 16 + 2 * c + uu) * 8);
}

__global__ __launch_bounds__(512, 2) void k_main(unsigned char* ws, uint16_t* out) {
  extern __shared__ __attribute__((aligned(16))) unsigned char smem[];
  LAS unsigned char* L = (LAS unsigned char*)smem;
  const int w = __builtin_amdgcn_readfirstlane(threadIdx.x >> 6);
  int g, c; blk_gc(blockIdx.x, g, c);
  const rsrc_t rs = __builtin_amdgcn_make_buffer_rsrc((void*)ws, 0, WS_TOTAL, 0x00020000);
  const rsrc_t rsnull = __builtin_amdgcn_make_buffer_rsrc((void*)ws, 0, 0, 0x00020000);
  const f32x4 zero = {0, 0, 0, 0};
  const int hd = 4 * c + (w & 3);
  int skv = OFF_WQKV + ((w < 4 ? 32 : 64) + hd) * 16 * KB;
  int sq_ = OFF_WQKV + hd * 16 * KB;
  int so_ = OFF_WO + (4 * c * 16 + 2 * w) * KB;
  int s1_ = OFF_W1 + (14 * c + 2 * (w < 7 ? w : 6)) * 16 * KB;
  int s2_ = OFF_W2 + (4 * c * 56 + 7 * w) * KB;
  int sx_ = w * 4096 + g * 64 * KB;
  int ss_ = w * 512 + g * 8 * KB;
  const rsrc_t rs1 = __builtin_amdgcn_make_buffer_rsrc((void*)ws, 0, w < 7 ? WS_TOTAL : 0, 0x00020000);
  const rsrc_t rsk = __builtin_amdgcn_make_buffer_rsrc((void*)ws, 0, w < 4 ? WS_TOTAL : 0, 0x00020000);
  STAMP_DECL
  STAMP(0);
#define LDO(i) ld128(rs, l16, so_ + (((i) >> 1) * 16 + ((i) & 1)) * KB)
#define LD1(i) ld128(rs1, l16, s1_ + (i) * KB)
#define LD2(i) ld128(rs, l16, s2_ + (((i) & 3) * 56 + ((i) >> 2)) * KB)
  v4u wkv[16], wqq[16];
  XS xs;
  {
    const int lane = fresh_lane(), l16 = lane * 16, tid = w * 64 + lane;
    x_issue(xs, rs, OFF_X0 + sx_, OFF_ST0 + ss_);
#pragma unroll
    for (int i = 0; i < 16; ++i) wkv[i] = ld128(rs, l16, skv + i * KB);
#pragma unroll
    for (int i = 0; i < 16; ++i) wqq[i] = ld128(rs, l16, sq_ + i * KB);
    if (w < 4) {
#pragma unroll
      for (int hf = 0; hf < 2; ++hf)
        *(LAS v4u*)(L + LDS_RES + (hf * 4 + w) * KB + l16) = ld128(rs, l16, OFF_X0 + (((g * 2 + hf) * 16 + 2 * c + (w & 1)) * 2 + (w >> 1)) * KB);
    }
    LAS float* cw = (LAS float*)(L + LDS_CS);
    if (tid < 192) cw[tid] = ((const float*)(ws + OFF_CSQ))[(tid >> 6) * 512 + 64 * c + (tid & 63)];
    else if (tid < 416) cw[tid] = ((const float*)(ws + OFF_CS1))[224 * c + tid - 192];
  }
  const LAS float* csl = (const LAS float*)(L + LDS_CS);
  SB();
#pragma unroll 1
  for (int l = 0; l < 2; ++l) {
    v4u wo[8], w1[32], w2[28];
    OPQ(skv); OPQ(sq_); OPQ(so_); OPQ(s1_); OPQ(s2_); OPQ(sx_); OPQ(ss_);
    {
      const int xo = (l == 0 ? OFF_X0 : OFF_X1 + XBYTES) + sx_, so = (l == 0 ? OFF_ST0 : OFF_ST1 + STBYTES) + ss_;
      v4u fr[2];
#pragma unroll
      for (int hf = 0; hf < 2; ++hf) {
        x_finish(xs, rs, xo + hf * 32 * KB, so + hf * 4 * KB, L, hf, w);
        const int lane = fresh_lane(), l16 = lane * 16, j = lane & 15, h = lane >> 4;
        const LAS v4u* xl = (const LAS v4u*)(L + hf * LDS_REG + l16);
        const LAS float* st = (const LAS float*)(L + LDS_STAT + hf * 256);
        v4u xr[4][2];
#pragma unroll
        for (int u = 0; u < 4; ++u) { xr[u][0] = xl[(u * 2) * 64]; xr[u][1] = xl[(u * 2 + 1) * 64]; }
        SB();
        f32x4 a[2] = {zero, zero};
#pragma unroll
        for (int u = 0; u < 16; ++u) {
          a[0] = mfma32(xr[u % 4][0], wkv[u], a[0]);
          a[1] = mfma32(xr[u % 4][1], wkv[u], a[1]);
          if (u + 4 < 16) { xr[u % 4][0] = xl[((u + 4) * 2) * 64]; xr[u % 4][1] = xl[((u + 4) * 2 + 1) * 64]; }
          SB();
        }
        if (hf == 0) x_issue(xs, rs, xo + 32 * KB, so + 4 * KB);
        SB();
        const float cs = csl[64 + 16 * w + j];
        unsigned f[4];
#pragma unroll
        for (int mm = 0; mm < 2; ++mm) {
          const f32x4 mean = *(const LAS f32x4*)(st + 16 * mm + 4 * h), rstd = *(const LAS f32x4*)(st + 32 + 16 * mm + 4 * h);
          float t[4];
#pragma unroll
          for (int r = 0; r < 4; ++r) { const float tt = (a[mm][r] - mean[r] * cs) * rstd[r]; t[r] = w < 4 ? phi(tt) : tt; }
          f[2 * mm] = pk2(t[0], t[1]); f[2 * mm + 1] = pk2(t[2], t[3]);
        }
        fr[hf] = v4u{f[0], f[1], f[2], f[3]};
      }
      const int lane = fresh_lane(), l16 = lane * 16, j = lane & 15, h = lane >> 4;
      LAS v4u* vx = (LAS v4u*)(L + LDS_VX + (w & 3) * 2 * KB + l16);
      if (w >= 4) { vx[0] = fr[0]; vx[64] = fr[1]; }
      __syncthreads();
      {
        f32x4 kv = zero, z = zero;
        if (w < 4) {
          const v4u ones = {0x3F803F80u, 0x3F803F80u, 0x3F803F80u, 0x3F803F80u};
          kv = mfma32(fr[0], vx[0], kv); z = mfma32(fr[0], ones, z);
          kv = mfma32(fr[1], vx[64], kv); z = mfma32(fr[1], ones, z);
        }
        asm volatile("" : "+v"(kv), "+v"(z));
        const v4u kvu = __builtin_bit_cast(v4u, kv), zu = __builtin_bit_cast(v4u, z);
        st128c(rsk, v4u{san(kvu.x), san(kvu.y), san(kvu.z), san(kvu.w)}, l16, OFF_KVP + l * KVPBYTES + (g * H + hd) * KB);
        st128c(rsk, v4u{san(zu.x), san(zu.y), san(zu.z), san(zu.w)}, j == 0 ? h * 16 : OOBV, OFF_ZP + l * ZPBYTES + (g * H + hd) * 64);
      }
      SB();
      {
        const int hf = w >> 2;
        const LAS v4u* xl = (const LAS v4u*)(L + hf * LDS_REG + l16);
        const LAS float* st = (const LAS float*)(L + LDS_STAT + hf * 256);
        v4u xr[4][2];
#pragma unroll
        for (int u = 0; u < 4; ++u) { xr[u][0] = xl[(u * 2) * 64]; xr[u][1] = xl[(u * 2 + 1) * 64]; }
        SB();
        f32x4 aq[2] = {zero, zero};
#pragma unroll
        for (int u = 0; u < 16; ++u) {
          aq[0] = mfma32(wqq[u], xr[u % 4][0], aq[0]);
          aq[1] = mfma32(wqq[u], xr[u % 4][1], aq[1]);
          if (u + 4 < 16) { xr[u % 4][0] = xl[((u + 4) * 2) * 64]; xr[u % 4][1] = xl[((u + 4) * 2 + 1) * 64]; }
          if ((u & 1) == 0) wo[u >> 1] = LDO(u >> 1);
          SB();
        }
        unsigned pq[4];
        const f32x4 csq = *(const LAS f32x4*)(csl + 16 * (w & 3) + 4 * h);
#pragma unroll
        for (int mm = 0; mm < 2; ++mm) {
          const float mean = st[16 * mm + j], rstd = st[32 + 16 * mm + j];
          float q[4];
#pragma unroll
          for (int r = 0; r < 4; ++r) q[r] = phi((aq[mm][r] - mean * csq[r]) * rstd);
          pq[2 * mm] = pk2s(q[0], q[1]); pq[2 * mm + 1] = pk2s(q[2], q[3]);
        }
        st128c(rs, v4u{pq[0], pq[1], pq[2], pq[3]}, l16, OFF_PQ + l * XBYTES + ((g * 2 + hf) * H + hd) * KB);
        SB();
      }
    }
    STAMP(1 + 6 * l);
    if (w == 4) {
      const int lane = fresh_lane();
      const int ch = lane & 7, gq = lane >> 3;
      v4u p[4]; unsigned n = 0;
      for (;;) {
#pragma unroll
        for (int i = 0; i < 4; ++i) p[i] = ld128c(rs, gq * H * KB + ch * 16, OFF_KVP + l * KVPBYTES + (i * 8 * H + g) * KB + c * 128);
        if (valid(umax(umax(umax4(p[0]), umax4(p[1])), umax(umax4(p[2]), umax4(p[3]))))) break;
        if (spin_fail(n, rs)) break;
      }
      f32x4 s = (__builtin_bit_cast(f32x4, p[0]) + __builtin_bit_cast(f32x4, p[1])) + (__builtin_bit_cast(f32x4, p[2]) + __builtin_bit_cast(f32x4, p[3]));
#pragma unroll
      for (int r = 0; r < 4; ++r) s[r] = sum_x32(sum_x16(ror_add<8>(s[r])));
      if (lane < 8) st64c(rs, v2u{pk2s(s.x, s.y), pk2s(s.z, s.w)}, lane * 16, OFF_KVR + l * KVRBYTES + ((g >> 1) * 64 + 8 * c) * 16 + (g & 1) * 8);
    } else if (w == 5 && c == 0) {
      const int lane = fresh_lane();
      const int hh = lane & 3, gq = lane >> 2;
      v4u p[2]; unsigned n = 0;
      for (;;) {
#pragma unroll
        for (int i = 0; i < 2; ++i) p[i] = ld128c(rs, gq * H * 64 + hh * 16, OFF_ZP + l * ZPBYTES + (i * 16 * H + g) * 64);
        if (valid(umax(umax4(p[0]), umax4(p[1])))) break;
        if (spin_fail(n, rs)) break;
      }
      f32x4 s = __builtin_bit_cast(f32x4, p[0]) + __builtin_bit_cast(f32x4, p[1]);
#pragma unroll
      for (int r = 0; r < 4; ++r) s[r] = sum_x32(sum_x16(ror_add<8>(ror_add<4>(s[r]))));
      if (lane < 4) st64c(rs, v2u{pk2s(s.x, s.y), pk2s(s.z, s.w)}, lane * 32, OFF_ZR + l * ZRBYTES + (g >> 2) * 128 + (g & 3) * 8);
    }
    {
      const int lane = fresh_lane(), l16 = lane * 16, h = lane >> 4;
      int sp_ = OFF_PQ + l * XBYTES + (g * 2 * H + 4 * w) * KB; OPQ(sp_);
#pragma unroll
      for (int u = 0; u < W1_EARLY; ++u) { w1[u] = LD1(u); w1[16 + u] = LD1(16 + u); }
      v4u pq[4];
#pragma unroll
      for (int hh = 0; hh < 4; ++hh) pq[hh] = ld128c(rs, l16, sp_ + hh * KB);
      SB();
      v4u kvf[2], zf[2];
      {
        unsigned n = 0;
        for (;;) {
          kvf[0] = ld128c(rs, l16, OFF_KVR + l * KVRBYTES + 2 * w * KB);
          kvf[1] = ld128c(rs, l16, OFF_KVR + l * KVRBYTES + (2 * w + 1) * KB);
          zf[0] = ld128c(rs, h * 32, OFF_ZR + l * ZRBYTES + w * 128);
          zf[1] = ld128c(rs, h * 32, OFF_ZR + l * ZRBYTES + w * 128 + 16);
          if (valid(umax(umax(umax4(kvf[0]), umax4(kvf[1])), umax(umax4(zf[0]), umax4(zf[1]))))) break;
          __builtin_amdgcn_s_sleep(KVR_SLEEP);
          if (spin_fail(n, rs)) break;
        }
      }
      STAMP(2 + 6 * l);
      __syncthreads();
      const int x1o = OFF_X1 + 2 * l * XBYTES + g * 64 * KB, s1o = OFF_ST1 + 2 * l * STBYTES + g * 8 * KB;
#pragma unroll
      for (int hf = 0; hf < 2; ++hf) {
        if (__builtin_expect(!valid(umax(umax(umax4(pq[0]), umax4(pq[1])), umax(umax4(pq[2]), umax4(pq[3])))), 0)) {
          unsigned n = 0;
          do {
            if (spin_fail(n, rs)) break;
#pragma unroll
            for (int hh = 0; hh < 4; ++hh) pq[hh] = ld128c(rs, l16, sp_ + (hf * H + hh) * KB);
          } while (!valid(umax(umax(umax4(pq[0]), umax4(pq[1])), umax(umax4(pq[2]), umax4(pq[3])))));
        }
        v4u of[2][2];
#pragma unroll
        for (int hh = 0; hh < 4; ++hh) {
          const v4u kk = kvf[hh >> 1], zz = zf[hh >> 1];
          const v2u ka = (hh & 1) ? v2u{kk.z, kk.w} : v2u{kk.x, kk.y};
          const v2u za = (hh & 1) ? v2u{zz.z, zz.w} : v2u{zz.x, zz.y};
#pragma unroll
          for (int mm = 0; mm < 2; ++mm) {
            const v2u b = mm ? v2u{pq[hh].z, pq[hh].w} : v2u{pq[hh].x, pq[hh].y};
            const f32x4 num = mfma16(ka, b, zero);
            const f32x4 den = mfma16(za, b, zero);
            const float rd = __builtin_amdgcn_rcpf(den.x + 1e-6f);
            const unsigned lo = pk2(num.x * rd, num.y * rd), hi = pk2(num.z * rd, num.w * rd);
            if (hh & 1) { of[hh >> 1][mm].z = lo; of[hh >> 1][mm].w = hi; } else { of[hh >> 1][mm].x = lo; of[hh >> 1][mm].y = hi; }
          }
        }
        SB();
        if (hf == 0) {
#pragma unroll
          for (int hh = 0; hh < 4; ++hh) pq[hh] = ld128c(rs, l16, sp_ + (H + hh) * KB);
        } else x_issue(xs, rs, OFF_X1 + 2 * l * XBYTES + sx_, OFF_ST1 + 2 * l * STBYTES + ss_);
        LAS f32x4* red = (LAS f32x4*)(L + hf * LDS_REG + w * 8 * KB + l16);
#pragma unroll
        for (int nt = 0; nt < 4; ++nt) {
#pragma unroll
          for (int mm = 0; mm < 2; ++mm) {
            f32x4 acc = mfma32(wo[nt * 2], of[0][mm], zero);
            acc = mfma32(wo[nt * 2 + 1], of[1][mm], acc);
            red[(nt * 2 + mm) * 64] = acc;
          }
        }
        __syncthreads();
        red_out<false>(rsk, rsk, L, hf, x1o + hf * 32 * KB, s1o + hf * 4 * KB, g, c, hf, w);
        if (hf == 1) {
#pragma unroll
          for (int u = W1_EARLY; u < 16; ++u) { w1[u] = LD1(u); w1[16 + u] = LD1(16 + u); }
        }
      }
    }
    STAMP(3 + 6 * l);
    v4u hfr[14];
    int sh_ = OFF_H + l * HBYTES + ((g * 2 * 56 + 7 * w) * 2) * KB; OPQ(sh_);
    {
      const int xo = OFF_X1 + 2 * l * XBYTES + sx_, so = OFF_ST1 + 2 * l * STBYTES + ss_;
#pragma unroll
      for (int hf = 0; hf < 2; ++hf) {
        x_finish(xs, rs, xo + hf * 32 * KB, so + hf * 4 * KB, L, hf, w);
        const int lane = fresh_lane(), l16 = lane * 16, j = lane & 15, h = lane >> 4;
        const LAS v4u* xl = (const LAS v4u*)(L + hf * LDS_REG + l16);
        const LAS float* st = (const LAS float*)(L + LDS_STAT + hf * 256);
        f32x4 a0[2] = {zero, zero}, a1[2] = {zero, zero};
        v4u xr[3][2];
#pragma unroll
        for (int u = 0; u < 3; ++u) { xr[u][0] = xl[(u * 2) * 64]; xr[u][1] = xl[(u * 2 + 1) * 64]; }
        SB();
#pragma unroll
        for (int u = 0; u < 16; ++u) {
          const v4u x0 = xr[u % 3][0], x1 = xr[u % 3][1];
          a0[0] = mfma32(w1[u], x0, a0[0]); a1[0] = mfma32(w1[16 + u], x0, a1[0]);
          a0[1] = mfma32(w1[u], x1, a0[1]); a1[1] = mfma32(w1[16 + u], x1, a1[1]);
          if (u + 3 < 16) { xr[u % 3][0] = xl[((u + 3) * 2) * 64]; xr[u % 3][1] = xl[((u + 3) * 2 + 1) * 64]; }
          if (hf == 0) { if ((u & 1) == 0 && (u >> 1) < W2_EARLY) w2[u >> 1] = LD2(u >> 1); }
          else { const int i0 = W2_EARLY + ((28 - W2_EARLY) * u) / 16, i1 = W2_EARLY + ((28 - W2_EARLY) * (u + 1)) / 16;
#pragma unroll
                 for (int i = i0; i < i1; ++i) w2[i] = LD2(i); }
          SB();
        }
        if (hf == 0) x_issue(xs, rs, xo + 32 * KB, so + 4 * KB);
        else {
#pragma unroll
          for (int i = 0; i < 14; ++i) hfr[i] = ld128c(rs, l16, sh_ + i * KB);
        }
        SB();
        {
          const f32x4 c0 = *(const LAS f32x4*)(csl + 192 + 32 * (w < 7 ? w : 6) + 4 * h), c1 = *(const LAS f32x4*)(csl + 192 + 32 * (w < 7 ? w : 6) + 16 + 4 * h);
#pragma unroll
          for (int mm = 0; mm < 2; ++mm) {
            const float mean = st[16 * mm + j], rstd = st[32 + 16 * mm + j];
            float t[8];
#pragma unroll
            for (int r = 0; r < 4; ++r) { t[r] = (a0[mm][r] - mean * c0[r]) * rstd; t[4 + r] = (a1[mm][r] - mean * c1[r]) * rstd; }
#pragma unroll
            for (int r = 0; r < 8; ++r) t[r] = t[r] * __builtin_amdgcn_rcpf(1.f + fexp(-t[r]));
            st128c(rs1, v4u{pk2s(t[0], t[1]), pk2s(t[2], t[3]), pk2s(t[4], t[5]), pk2s(t[6], t[7])}, l16,
                   OFF_H + l * HBYTES + (((g * 2 + hf) * 56 + 7 * c + w) * 2 + mm) * KB);
          }
        }
        SB();
      }
    }
    STAMP(4 + 6 * l);
    {
      const rsrc_t rsn = __builtin_amdgcn_make_buffer_rsrc((void*)ws, 0, l == 0 ? WS_TOTAL : 0, 0x00020000);
      const rsrc_t rsx = __builtin_amdgcn_make_buffer_rsrc((void*)ws, 0, (l == 0 && w < 4) ? WS_TOTAL : 0, 0x00020000);
      const rsrc_t rso = __builtin_amdgcn_make_buffer_rsrc((void*)out, 0, (l == 1 && w < 4) ? S * D * 2 : 0, 0x00020000);
      const int x2o = OFF_X1 + XBYTES + g * 64 * KB, s2o = OFF_ST1 + STBYTES + g * 8 * KB;
#pragma unroll
      for (int hf = 0; hf < 2; ++hf) {
        const int l16 = fresh_lane() * 16;
        if (__builtin_expect(!valid(hmax(hfr)), 0)) {
          unsigned n = 0;
          do {
            if (spin_fail(n, rs)) break;
#pragma unroll
            for (int i = 0; i < 14; ++i) hfr[i] = ld128c(rs, l16, sh_ + (hf * 112 + i) * KB);
          } while (!valid(hmax(hfr)));
        }
        if (hf == 0) STAMP(5 + 6 * l);
        f32x4 acc[4][2];
#pragma unroll
        for (int nt = 0; nt < 4; ++nt) { acc[nt][0] = zero; acc[nt][1] = zero; }
#pragma unroll
        for (int u = 0; u < 7; ++u) {
#pragma unroll
          for (int nt = 0; nt < 4; ++nt) {
            acc[nt][0] = mfma32(w2[u * 4 + nt], hfr[u * 2], acc[nt][0]);
            acc[nt][1] = mfma32(w2[u * 4 + nt], hfr[u * 2 + 1], acc[nt][1]);
          }
          if (hf == 1) {
#pragma unroll
            for (int t = 0; t < 2; ++t) wkv[2 * u + t] = ld128(rsn, l16, skv + (2 * u + t) * KB);
#pragma unroll
            for (int t = 0; t < 2; ++t) wqq[2 * u + t] = ld128(rsn, l16, sq_ + (2 * u + t) * KB);
          }
          SB();
        }
        if (hf == 0) {
        } else {
#pragma unroll
          for (int i = 14; i < 16; ++i) { wkv[i] = ld128(rsn, l16, skv + i * KB); wqq[i] = ld128(rsn, l16, sq_ + i * KB); }
          x_issue(xs, rsn, OFF_X1 + XBYTES + sx_, OFF_ST1 + STBYTES + ss_);
        }
        red_write(acc, L, hf, w);
        __syncthreads();
        if (hf == 0) {
#pragma unroll
          for (int i = 0; i < 14; ++i) hfr[i] = ld128c(rs, l16, sh_ + (112 + i) * KB);
        }
        red_out<true>(rsx, rso, L, hf, x2o + hf * 32 * KB, s2o + hf * 4 * KB, g, c, hf, w);
      }
    }
    STAMP(6 + 6 * l);
  }
  STAMP_FLUSH;
}
}

extern "C" void kernel_launch(void* const* d_in, const int* in_sizes, int n_in, void* d_out, int out_size, void* d_ws, size_t ws_size, hipStream_t stream) {
  const uint16_t* x = (const uint16_t*)d_in[0];
  const uint16_t* wqkv = (const uint16_t*)d_in[1];
  const uint16_t* wo = (const uint16_t*)d_in[2];
  const uint16_t* w1 = (const uint16_t*)d_in[4];
  const uint16_t* w2 = (const uint16_t*)d_in[5];
  unsigned char* ws = (unsigned char*)d_ws;
  uint16_t* out = (uint16_t*)d_out;
  (void)hipFuncSetAttribute((const void*)k_main, hipFuncAttributeMaxDynamicSharedMemorySize, LDS_BYTES);
  k_prep<<<312, 256, 0, stream>>>(x, wqkv, wo, w1, w2, ws);
  k_main<<<256, 512, LDS_BYTES, stream>>>(ws, out);
}
```

```cpp
#include <hip/hip_runtime.h>
#include <hip/hip_bf16.h>
#include <stdint.h>

#define GAS __attribute__((address_space(1)))
#define LAS __attribute__((address_space(3)))
typedef unsigned v4u __attribute__((ext_vector_type(4)));
typedef unsigned v2u __attribute__((ext_vector_type(2)));
typedef float f32x4 __attribute__((ext_vector_type(4)));
typedef float f32x2 __attribute__((ext_vector_type(2)));
typedef __bf16 bf16x8 __attribute__((ext_vector_type(8)));
typedef __bf16 bf16x2 __attribute__((ext_vector_type(2)));
typedef short s16x4 __attribute__((ext_vector_type(4)));

#define STAMP(slot)
#define STAMP_DECL
#define STAMP_FLUSH

namespace {
constexpr int S = 2048, D = 512, F = 1792, H = 32;
constexpr int NG = 32;
constexpr int KB = 1024;
constexpr unsigned SENT = 0xFFFFFFFFu;
constexpr int OFF_WQKV = 0;
constexpr int OFF_WO = OFF_WQKV + 96 * 16 * KB;
constexpr int OFF_W1 = OFF_WO + 32 * 16 * KB;
constexpr int OFF_W2 = OFF_W1 + 112 * 16 * KB;
constexpr int OFF_CSQ = OFF_W2 + 32 * 56 * KB;
constexpr int OFF_CS1 = OFF_CSQ + 8 * KB;
constexpr int OFF_TMO = OFF_CS1 + 8 * KB;
constexpr int XBYTES = NG * 64 * KB;
constexpr int STBYTES = NG * 8 * KB;
constexpr int OFF_X0 = OFF_TMO + 4 * KB;
constexpr int OFF_ST0 = OFF_X0 + XBYTES;
constexpr int OFF_FILL0 = OFF_ST0 + STBYTES;
constexpr int KVPBYTES = NG * H * KB;
constexpr int OFF_KVP = OFF_FILL0;
constexpr int ZPBYTES = NG * H * 64;
constexpr int OFF_ZP = OFF_KVP + 2 * KVPBYTES;
constexpr int KVRBYTES = 16 * KB;
constexpr int OFF_KVR = OFF_ZP + 2 * ZPBYTES;
constexpr int ZRBYTES = KB;
constexpr int OFF_ZR = OFF_KVR + 2 * KVRBYTES;
constexpr int OFF_ST1 = OFF_ZR + 2 * ZRBYTES;
constexpr int OFF_PQ0 = OFF_ST1 + 3 * STBYTES;
constexpr int OFF_X1 = OFF_PQ0 + XBYTES;
#define FILL_MAIN 0
constexpr int OFF_PQ1 = OFF_X1 + 3 * XBYTES;
constexpr int HBYTES = NG * 224 * KB;
constexpr int OFF_H = OFF_PQ1 + XBYTES;
constexpr int WS_TOTAL = OFF_H + 2 * HBYTES;
constexpr int OFF_FILL1 = FILL_MAIN ? OFF_X1 + XBYTES : WS_TOTAL;
static_assert((OFF_FILL0 % 1024) == 0 && (OFF_FILL1 % 1024) == 0, "");
__host__ __device__ constexpr int xoff(int xi) { return xi == 0 ? OFF_X0 : OFF_X1 + (xi - 1) * XBYTES; }
__host__ __device__ constexpr int stoff(int xi) { return xi == 0 ? OFF_ST0 : OFF_ST1 + (xi - 1) * STBYTES; }

__device__ __forceinline__ f32x4 mfma32(v4u a, v4u b, f32x4 c) {
  return __builtin_amdgcn_mfma_f32_16x16x32_bf16(__builtin_bit_cast(bf16x8, a), __builtin_bit_cast(bf16x8, b), c, 0, 0, 0);
}
__device__ __forceinline__ f32x4 mfma16(v2u a, v2u b, f32x4 c) {
  return __builtin_amdgcn_mfma_f32_16x16x16bf16_1k(__builtin_bit_cast(s16x4, a), __builtin_bit_cast(s16x4, b), c, 0, 0, 0);
}
__device__ __forceinline__ unsigned pk2(float lo, float hi) {
  bf16x2 v; v.x = (__bf16)lo; v.y = (__bf16)hi;
  return __builtin_bit_cast(unsigned, v);
}
__device__ __forceinline__ unsigned san(unsigned u) { return u < 0xFFFFFFFEu ? u : 0xFFFFFFFEu; }
__device__ __forceinline__ unsigned pk2s(float lo, float hi) { return san(pk2(lo, hi)); }
__device__ __forceinline__ float asf(unsigned u) { return __builtin_bit_cast(float, u); }
__device__ __forceinline__ float bflo(unsigned u) { return __builtin_bit_cast(float, u << 16); }
__device__ __forceinline__ float bfhi(unsigned u) { return __builtin_bit_cast(float, u & 0xFFFF0000u); }
__device__ __forceinline__ float fexp(float t) { return __builtin_amdgcn_exp2f(t * 1.44269504088896f); }
__device__ __forceinline__ float phi(float t) { return fmaxf(t, 0.f) + fexp(fminf(t, 0.f)) + 1e-6f; }
__device__ __forceinline__ int permk(int h, int i) { return i < 4 ? 4 * h + i : 16 + 4 * h + (i - 4); }
#define XMAP 2
__device__ __forceinline__ void blk_gc(int b, int& g, int& c) {
  const int xcd = b & 7, idx = b >> 3;
  if (XMAP == 1) { g = xcd * 4 + (idx >> 3); c = idx & 7; }
  else if (XMAP == 2) { g = (xcd >> 1) * 8 + (idx >> 2); c = (xcd & 1) * 4 + (idx & 3); }
  else { g = (xcd >> 2) * 16 + (idx >> 1); c = (xcd & 3) * 2 + (idx & 1); }
}
__device__ __forceinline__ unsigned umax(unsigned a, unsigned b) { return a > b ? a : b; }
__device__ __forceinline__ unsigned umax4(v4u v) { return umax(umax(v.x, v.y), umax(v.z, v.w)); }
#define SB() __builtin_amdgcn_sched_barrier(0)

constexpr int PREP_BLOCKS = 352;
__global__ __launch_bounds__(256) void k_prep(const uint16_t* __restrict__ wqkv, const uint16_t* __restrict__ wo,
                                              const uint16_t* __restrict__ w1, const uint16_t* __restrict__ w2, unsigned char* __restrict__ ws) {
  __shared__ __attribute__((aligned(16))) unsigned char sm[4 * 32 * 144];
  const int tid = threadIdx.x, lane = tid & 63, w = __builtin_amdgcn_readfirstlane(tid >> 6);
  int id = blockIdx.x * 4 + w;
  const uint16_t* W; int N, off, UT, u, ng;
  if (id < 384) { W = wqkv; N = 1536; off = OFF_WQKV; UT = 16; u = id & 15; ng = id >> 4; }
  else if (id < 512) { id -= 384; W = wo; N = 512; off = OFF_WO; UT = 16; u = id & 15; ng = id >> 4; }
  else if (id < 960) { id -= 512; W = w1; N = 1792; off = OFF_W1; UT = 16; u = id & 15; ng = id >> 4; }
  else { id -= 960; W = w2; N = 512; off = OFF_W2; UT = 56; u = id % 56; ng = id / 56; }
  v4u v[4];
#pragma unroll
  for (int q = 0; q < 4; ++q) v[q] = *(const v4u*)(W + (size_t)(32 * u + 8 * q + (lane >> 3)) * N + ng * 64 + (lane & 7) * 8);
  {
    const int n16 = (OFF_FILL1 - OFF_FILL0) / 16;
    v4u* p = (v4u*)(ws + OFF_FILL0);
    for (int i = blockIdx.x * 256 + tid; i < n16; i += PREP_BLOCKS * 256) p[i] = v4u{SENT, SENT, SENT, SENT};
    if (blockIdx.x == 0 && tid == 0) *(unsigned*)(ws + OFF_TMO) = 0u;
  }
  LAS unsigned char* S = (LAS unsigned char*)sm + w * (32 * 144);
#pragma unroll
  for (int q = 0; q < 4; ++q) *(LAS v4u*)(S + (8 * q + (lane >> 3)) * 144 + (lane & 7) * 16) = v[q];
  const int h = lane >> 4, qq = (lane & 15) >> 2, p = lane & 3;
#pragma unroll
  for (int t = 0; t < 4; ++t) {
    const s16x4 lo = __builtin_amdgcn_ds_read_tr16_b64_v4i16((LAS s16x4*)(S + (4 * h + qq) * 144 + (16 * t + 4 * p) * 2));
    const s16x4 hi = __builtin_amdgcn_ds_read_tr16_b64_v4i16((LAS s16x4*)(S + (16 + 4 * h + qq) * 144 + (16 * t + 4 * p) * 2));
    const v2u a = __builtin_bit_cast(v2u, lo), b = __builtin_bit_cast(v2u, hi);
    *(v4u*)(ws + off + ((size_t)((ng * 4 + t) * UT + u) * 64 + lane) * 16) = v4u{a.x, a.y, b.x, b.y};
  }
}

typedef __amdgpu_buffer_rsrc_t rsrc_t;
constexpr int SC1 = 16;
__device__ __forceinline__ v4u ld128(rsrc_t rs, int voff, int soff) { return __builtin_amdgcn_raw_buffer_load_b128(rs, voff, soff, 0); }
__device__ __forceinline__ v4u ld128c(rsrc_t rs, int voff, int soff) { return __builtin_amdgcn_raw_buffer_load_b128(rs, voff, soff, SC1); }
__device__ __forceinline__ v2u ld64c(rsrc_t rs, int voff, int soff) { return __builtin_amdgcn_raw_buffer_load_b64(rs, voff, soff, SC1); }
__device__ __forceinline__ unsigned ld32c(rsrc_t rs, int voff, int soff) { return __builtin_amdgcn_raw_buffer_load_b32(rs, voff, soff, SC1); }
__device__ __forceinline__ void st128c(rsrc_t rs, v4u v, int voff, int soff) { __builtin_amdgcn_raw_buffer_store_b128(v, rs, (int)((unsigned)voff + (unsigned)soff), 0, SC1); }
__device__ __forceinline__ void st64c(rsrc_t rs, v2u v, int voff, int soff) { __builtin_amdgcn_raw_buffer_store_b64(v, rs, voff, soff, SC1); }
__device__ __forceinline__ void st32c(rsrc_t rs, unsigned v, int voff, int soff) { __builtin_amdgcn_raw_buffer_store_b32(v, rs, voff, soff, SC1); }
__device__ __forceinline__ bool valid(unsigned m) { return __builtin_amdgcn_ballot_w64(m == SENT) == 0ull; }
__device__ __forceinline__ unsigned asu(float f) { return __builtin_bit_cast(unsigned, f); }
template <int N> __device__ __forceinline__ float ror_add(float x) {
  return x + asf((unsigned)__builtin_amdgcn_update_dpp(0, (int)asu(x), 0x120 | N, 0xF, 0xF, false));
}
__device__ __forceinline__ float sum_x16(float x) { auto r = __builtin_amdgcn_permlane16_swap(asu(x), asu(x), false, false); return asf(r[0]) + asf(r[1]); }
__device__ __forceinline__ float sum_x32(float x) { auto r = __builtin_amdgcn_permlane32_swap(asu(x), asu(x), false, false); return asf(r[0]) + asf(r[1]); }
constexpr unsigned SPIN_CAP = 1u << 14;
__device__ __forceinline__ bool spin_fail(unsigned& n, rsrc_t rs) {
  asm volatile("" ::: "memory");
  __builtin_amdgcn_s_sleep(2);
  ++n;
  if (n > SPIN_CAP) { st32c(rs, 1u, OFF_TMO, 0); return true; }
  if ((n & 63u) == 0u) { if (__builtin_amdgcn_readfirstlane(ld32c(rs, OFF_TMO, 0)) != 0) return true; }
  return false;
}

#define DUTY_POS 0
#define W1_G 2
#define POLL_K0 2
#define PQ_K 3
#define WKV_LOOP 10
#define PQ_SLEEP 32
#define PQ_POS 1
#define X1_POS 2
#define WQQ_POS 2
#define H1_POS 1
#define H1_U 3
#define W1_Q 6
#define XI_U 16
#define W1_EARLY 14
#define KVR_SLEEP 4
#define W1_MID 6
#define W2_EARLY 8
#define W2_WO 0
constexpr int LDS_REG = 64 * KB;
constexpr int LDS_STAT = 2 * LDS_REG;
constexpr int LDS_CS = LDS_STAT + 512;
constexpr int LDS_RES = LDS_CS + 2048;
constexpr int LDS_VX = LDS_RES + 8 * KB;
constexpr int LDS_KV = LDS_VX;
constexpr int LDS_BYTES = LDS_VX + 17 * KB;
constexpr int OOBV = 0x7FFF0000;

__device__ __forceinline__ int fresh_lane() {
  unsigned z;
  asm volatile("s_mov_b32 %0, 0" : "=s"(z));
  return (int)__builtin_amdgcn_mbcnt_hi(~0u, __builtin_amdgcn_mbcnt_lo(~0u, z));
}
#define OPQ(x) asm volatile("" : "+s"(x))

__device__ __forceinline__ unsigned hmax(const v4u (&a)[14]) {
  unsigned m = 0;
#pragma unroll
  for (int i = 0; i < 14; ++i) m = umax(m, umax4(a[i]));
  return m;
}
__device__ __forceinline__ void reld128(v4u& a, const unsigned char* base, unsigned off) {
  asm volatile("s_nop 4\n\tglobal_load_dwordx4 %0, %1, %2 sc1" : "+v"(a) : "v"(off), "s"(base) : "memory");
}
__device__ __forceinline__ void reld64(v2u& a, const unsigned char* base, unsigned off) {
  asm volatile("s_nop 4\n\tglobal_load_dwordx2 %0, %1, %2 sc1" : "+v"(a) : "v"(off), "s"(base) : "memory");
}
struct XS { v4u x[4]; v2u s; };
__device__ __forceinline__ void x_issue(XS& a, rsrc_t rs, int xo, int so) {
  const int ln = fresh_lane();
#pragma unroll
  for (int i = 0; i < 4; ++i) a.x[i] = ld128c(rs, ln * 16 + i * KB, xo);
  a.s = ld64c(rs, ln * 8, so);
}
__device__ __forceinline__ unsigned x_max(const XS& a) { return umax(umax(umax4(a.x[0]), umax4(a.x[1])), umax(umax(umax4(a.x[2]), umax4(a.x[3])), umax(a.s.x, a.s.y))); }
__device__ __forceinline__ void x_finish(XS& a, rsrc_t rs, const unsigned char* ws, int xo, int so, LAS unsigned char* L, int par, int w) {
  if (__builtin_expect(!valid(x_max(a)), 0)) {
    unsigned n = 0;
    do {
      if (spin_fail(n, rs)) break;
      const unsigned ln = (unsigned)fresh_lane();
#pragma unroll
      for (int i = 0; i < 4; ++i) reld128(a.x[i], ws, ln * 16 + xo + i * KB);
      reld64(a.s, ws, ln * 8 + so);
      asm volatile("s_waitcnt vmcnt(0)" : "+v"(a.x[0]), "+v"(a.x[1]), "+v"(a.x[2]), "+v"(a.x[3]), "+v"(a.s) :: "memory");
    } while (!valid(x_max(a)));
  }
  const int ln = fresh_lane();
  LAS unsigned char* dst = L + par * LDS_REG + w * 4096 + ln * 16;
#pragma unroll
  for (int i = 0; i < 4; ++i) *(LAS v4u*)(dst + i * KB) = a.x[i];
  const float mp = asf(a.s.x), m2p = asf(a.s.y);
  const float mean = ror_add<1>(ror_add<2>(ror_add<4>(ror_add<8>(mp)))) * (1.f / 16.f);
  const float dm = mp - mean;
  const float m2 = ror_add<1>(ror_add<2>(ror_add<4>(ror_add<8>(m2p + 32.f * dm * dm))));
  const float rstd = __builtin_amdgcn_rsqf(m2 * (1.f / 512.f) + 1e-5f);
  if ((ln & 15) == 0) {
    LAS float* st = (LAS float*)(L + LDS_STAT + par * 256) + w * 4 + (ln >> 4);
    st[0] = mean; st[32] = rstd;
  }
  __syncthreads();
}

__device__ __forceinline__ void xraw_issue(XS& a, rsrc_t rx, int row0) {
  const int ln = fresh_lane();
#pragma unroll
  for (int i = 0; i < 4; ++i) a.x[i] = ld128(rx, ln * 16 + i * KB, row0 * KB);
}
__device__ __forceinline__ float wave_sum(float x) { return sum_x32(sum_x16(ror_add<1>(ror_add<2>(ror_add<4>(ror_add<8>(x)))))); }
__device__ __forceinline__ void xraw_finish(XS& a, LAS unsigned char* L, int par, int w) {
  const int ln = fresh_lane();
  const int u = ln >> 2, q = ln & 3;
#pragma unroll
  for (int i = 0; i < 4; ++i) {
    const int tok = 4 * w + i;
    LAS unsigned char* d = L + par * LDS_REG + (u * 2 + (tok >> 4)) * KB + ((tok & 15) + 32 * (q & 1)) * 16 + (q >> 1) * 8;
    *(LAS v2u*)d = v2u{a.x[i].x, a.x[i].y};
    *(LAS v2u*)(d + 256) = v2u{a.x[i].z, a.x[i].w};
    const float v[8] = {bflo(a.x[i].x), bfhi(a.x[i].x), bflo(a.x[i].y), bfhi(a.x[i].y), bflo(a.x[i].z), bfhi(a.x[i].z), bflo(a.x[i].w), bfhi(a.x[i].w)};
    const float mean = wave_sum(((v[0] + v[1]) + (v[2] + v[3])) + ((v[4] + v[5]) + (v[6] + v[7]))) * (1.f / 512.f);
    float m2 = 0.f;
#pragma unroll
    for (int e = 0; e < 8; ++e) { const float dd = v[e] - mean; m2 += dd * dd; }
    m2 = wave_sum(m2);
    if (ln == 0) {
      LAS float* st = (LAS float*)(L + LDS_STAT + par * 256) + tok;
      st[0] = mean; st[32] = __builtin_amdgcn_rsqf(m2 * (1.f / 512.f) + 1e-5f);
    }
  }
  __syncthreads();
}

__device__ __forceinline__ void red_write(f32x4 (&acc)[4][2], LAS unsigned char* L, int par, int w) {
  const int ln = fresh_lane();
  LAS f32x4* red = (LAS f32x4*)(L + par * LDS_REG + w * 8 * KB + ln * 16);
#pragma unroll
  for (int nt = 0; nt < 4; ++nt)
#pragma unroll
    for (int mm = 0; mm < 2; ++mm) red[(nt * 2 + mm) * 64] = acc[nt][mm];
}
template <bool LAST>
__device__ __forceinline__ void red_out(rsrc_t rsw, rsrc_t rso, LAS unsigned char* L, int par, int xo, int so, int g, int c, int hf, int w) {
  const int ln = fresh_lane();
  const int mm = (w >> 1) & 1, uu = w & 1;
  const int j = ln & 15, h = ln >> 4;
  v4u o = {0, 0, 0, 0};
  v2u stv = {0, 0};
  if (w < 4) {
    const LAS f32x4* red = (const LAS f32x4*)(L + par * LDS_REG + (4 * uu + mm) * KB + ln * 16);
    LAS v4u* resp = (LAS v4u*)(L + LDS_RES + (hf * 4 + w) * KB + ln * 16);
    const v4u resid = *resp;
    f32x4 s0 = {0, 0, 0, 0}, s1 = {0, 0, 0, 0};
#pragma unroll
    for (int ww = 0; ww < 8; ++ww) {
      s0 += red[ww * 512];
      s1 += red[ww * 512 + 128];
    }
    s0.x += bflo(resid.x); s0.y += bfhi(resid.x); s0.z += bflo(resid.y); s0.w += bfhi(resid.y);
    s1.x += bflo(resid.z); s1.y += bfhi(resid.z); s1.z += bflo(resid.w); s1.w += bfhi(resid.w);
    o = v4u{pk2s(s0.x, s0.y), pk2s(s0.z, s0.w), pk2s(s1.x, s1.y), pk2s(s1.z, s1.w)};
    *resp = o;
  }
  asm volatile("" : "+v"(o));
  st128c(rsw, o, ln * 16, xo + ((2 * c + uu) * 2 + mm) * KB);
  if constexpr (LAST) {
    const int voff = j * (D * 2) + h * 8, soff = ((g * 64 + hf * 32 + 16 * mm) * D + 64 * c + 32 * uu) * 2;
    __builtin_amdgcn_raw_buffer_store_b64(v2u{o.x, o.y}, rso, voff, soff, 0);
    __builtin_amdgcn_raw_buffer_store_b64(v2u{o.z, o.w}, rso, voff, soff + 32, 0);
  }
  if (w < 4) {
    float v[8] = {bflo(o.x), bfhi(o.x), bflo(o.y), bfhi(o.y), bflo(o.z), bfhi(o.z), bflo(o.w), bfhi(o.w)};
    const float s = sum_x32(sum_x16(((v[0] + v[1]) + (v[2] + v[3])) + ((v[4] + v[5]) + (v[6] + v[7]))));
    const float mean = s * (1.f / 32.f);
    float m2 = 0.f;
#pragma unroll
    for (int i = 0; i < 8; ++i) { float d = v[i] - mean; m2 += d * d; }
    m2 = sum_x32(sum_x16(m2));
    stv = v2u{san(asu(mean)), san(asu(m2))};
  }
  asm volatile("" : "+v"(stv));
  st64c(rsw, stv, h == 0 ? j * 128 : OOBV, so + (16 * mm * 16 + 2 * c + uu) * 8);
}

__global__ __launch_bounds__(512, 2) void k_main(unsigned char* ws, const uint16_t* xin, uint16_t* out) {
  extern __shared__ __attribute__((aligned(16))) unsigned char smem[];
  LAS unsigned char* L = (LAS unsigned char*)smem;
  const int w = __builtin_amdgcn_readfirstlane(threadIdx.x >> 6);
  int g, c; blk_gc(blockIdx.x, g, c);
  const rsrc_t rs = __builtin_amdgcn_make_buffer_rsrc((void*)ws, 0, WS_TOTAL, 0x00020000);
  const rsrc_t rx = __builtin_amdgcn_make_buffer_rsrc((void*)xin, 0, S * D * 2, 0x00020000);
  const rsrc_t rsnull = __builtin_amdgcn_make_buffer_rsrc((void*)ws, 0, 0, 0x00020000);
  const f32x4 zero = {0, 0, 0, 0};
  const int hd = 4 * c + (w & 3);
  int skv = OFF_WQKV + ((w < 4 ? 32 : 64) + hd) * 16 * KB;
  int sq_ = OFF_WQKV + hd * 16 * KB;
  int so_ = OFF_WO + (4 * c * 16 + 2 * w) * KB;
  int s1_ = OFF_W1 + (14 * c + 2 * (w < 7 ? w : 6)) * 16 * KB;
  int s2_ = OFF_W2 + (4 * c * 56 + 7 * w) * KB;
  int sx_ = w * 4096 + g * 64 * KB;
  int ss_ = w * 512 + g * 8 * KB;
  const rsrc_t rs1 = __builtin_amdgcn_make_buffer_rsrc((void*)ws, 0, w < 7 ? WS_TOTAL : 0, 0x00020000);
  const rsrc_t rsk = __builtin_amdgcn_make_buffer_rsrc((void*)ws, 0, w < 4 ? WS_TOTAL : 0, 0x00020000);
  STAMP_DECL
  STAMP(0);
#define LDO(i) ld128(rs, l16 + ((i) & 1) * KB, so_ + ((i) >> 1) * 16 * KB)
#define LD1(i) ld128(rs, l16 + ((i) & 3) * KB, s1_ + ((i) >> 2) * 4 * KB)
#define LD1N(i) ld128(rs1, l16 + ((i) & 3) * KB, s1_ + ((i) >> 2) * 4 * KB)
#define LD2(i) ld128(rs, l16 + (((i) >> 2) & 3) * KB, s2_ + (((i) & 3) * 56 + (((i) >> 2) & 4)) * KB)
#define LDK(r_, i) ld128(r_, l16 + ((i) & 3) * KB, skv + ((i) >> 2) * 4 * KB)
#define LDH(i) ld128c(rs, l16 + ((i) & 3) * KB, sh_ + ((i) >> 2) * 4 * KB)
  v4u wkv[16];
  XS xs;
  {
    const int lane = fresh_lane(), l16 = lane * 16;
    xraw_issue(xs, rx, g * 64 + 4 * w);
#pragma unroll
    for (int i = 0; i < 16; ++i) wkv[i] = LDK(rs, i);
    if (w < 4) {
#pragma unroll
      for (int hf = 0; hf < 2; ++hf) {
        const int vo_ = (lane & 15) * KB + (lane >> 4) * 8, so2 = (g * 64 + hf * 32 + 16 * (w >> 1)) * KB + (64 * c + 32 * (w & 1)) * 2;
        const v2u a = __builtin_amdgcn_raw_buffer_load_b64(rx, vo_, so2, 0), b = __builtin_amdgcn_raw_buffer_load_b64(rx, vo_ + 32, so2, 0);
        *(LAS v4u*)(L + LDS_RES + (hf * 4 + w) * KB + l16) = v4u{a.x, a.y, b.x, b.y};
      }
    }
  }
  LAS float* csl = (LAS float*)(L + LDS_CS);
  const v4u ones4 = {0x3F803F80u, 0x3F803F80u, 0x3F803F80u, 0x3F803F80u};
  auto fill_group = [&](rsrc_t rf, rsrc_t rf7, rsrc_t rf4, int layer) {
    const int l16 = fresh_lane() * 16;
    const v4u sv = {SENT, SENT, SENT, SENT};
#pragma unroll
    for (int i = 0; i < 4; ++i) st128c(rf7, sv, l16, OFF_H + layer * HBYTES + (((g * 2 + (i >> 1)) * 56 + 7 * c + w) * 2 + (i & 1)) * KB);
#pragma unroll
    for (int hf = 0; hf < 2; ++hf) st128c(rf4, sv, l16, OFF_X1 + (1 + layer) * XBYTES + (g * 2 + hf) * 32 * KB + ((2 * c + (w & 1)) * 2 + ((w >> 1) & 1)) * KB);
    if (layer == 0) st128c(rf, sv, l16, OFF_PQ1 + ((g * 2 + (w >> 2)) * H + hd) * KB);
  };
  SB();
#pragma unroll 1
  for (int l = 0; l < 2; ++l) {
    v4u wqq[16], wo[8], w1[32], w2[28];
    OPQ(skv); OPQ(sq_); OPQ(so_); OPQ(s1_); OPQ(s2_); OPQ(sx_); OPQ(ss_);
    {
      const int xo = OFF_X1 + XBYTES + sx_, so = OFF_ST1 + STBYTES + ss_;
      v4u fr[2];
#pragma unroll
      for (int hf = 0; hf < 2; ++hf) {
        if (l == 0) {
          if (hf == 0) {
            f32x4 cs = zero;
#pragma unroll
            for (int u = 0; u < 16; ++u) cs = mfma32(ones4, wkv[u], cs);
            const int ln = fresh_lane();
            if (ln < 16) csl[64 + 16 * w + ln] = cs.x;
          }
          xraw_finish(xs, L, hf, w);
        } else x_finish(xs, rs, ws, xo + hf * 32 * KB, so + hf * 4 * KB, L, hf, w);
        const int lane = fresh_lane(), l16 = lane * 16, j = lane & 15, h = lane >> 4;
        const LAS v4u* xl = (const LAS v4u*)(L + hf * LDS_REG + l16);
        const LAS float* st = (const LAS float*)(L + LDS_STAT + hf * 256);
        v4u xr[4][2];
#pragma unroll
        for (int u = 0; u < 4; ++u) { xr[u][0] = xl[(u * 2) * 64]; xr[u][1] = xl[(u * 2 + 1) * 64]; }
        SB();
        f32x4 a[2] = {zero, zero};
#pragma unroll
        for (int u = 0; u < 16; ++u) {
          a[0] = mfma32(xr[u % 4][0], wkv[u], a[0]);
          a[1] = mfma32(xr[u % 4][1], wkv[u], a[1]);
          if (u + 4 < 16) { xr[u % 4][0] = xl[((u + 4) * 2) * 64]; xr[u % 4][1] = xl[((u + 4) * 2 + 1) * 64]; }
          if (hf == 0 && u == XI_U) { if (l == 0) xraw_issue(xs, rx, g * 64 + 32 + 4 * w); else x_issue(xs, rs, xo + 32 * KB, so + 4 * KB); }
          if (WQQ_POS == 1 && (u & 1) == 1) wqq[hf * 8 + (u >> 1)] = ld128(rs, l16 + ((u >> 1) & 3) * KB, sq_ + (hf * 8 + ((u >> 1) & 4)) * KB);
          SB();
        }
        if (hf == 0 && XI_U == 16) { if (l == 0) xraw_issue(xs, rx, g * 64 + 32 + 4 * w); else x_issue(xs, rs, xo + 32 * KB, so + 4 * KB); }
        if (WQQ_POS == 2) {
#pragma unroll
          for (int i = 0; i < 8; ++i) wqq[hf * 8 + i] = ld128(rs, l16 + (i & 3) * KB, sq_ + (hf * 8 + (i & 4)) * KB);
        }
        if (hf == 1) {
#pragma unroll
          for (int i = 0; i < 8; ++i) wo[i] = LDO(i);
        }
        if (FILL_MAIN && hf == 0) {
          const int nf = l == 0 ? WS_TOTAL : 0;
          fill_group(__builtin_amdgcn_make_buffer_rsrc((void*)ws, 0, nf, 0x00020000), __builtin_amdgcn_make_buffer_rsrc((void*)ws, 0, w < 7 ? nf : 0, 0x00020000),
                     __builtin_amdgcn_make_buffer_rsrc((void*)ws, 0, w < 4 ? nf : 0, 0x00020000), 0);
        }
        const float cs = csl[64 + 16 * w + j];
        unsigned f[4];
#pragma unroll
        for (int mm = 0; mm < 2; ++mm) {
          const f32x4 mean = *(const LAS f32x4*)(st + 16 * mm + 4 * h), rstd = *(const LAS f32x4*)(st + 32 + 16 * mm + 4 * h);
          float t[4];
#pragma unroll
          for (int r = 0; r < 4; ++r) { const float tt = (a[mm][r] - mean[r] * cs) * rstd[r]; t[r] = w < 4 ? phi(tt) : tt; }
          f[2 * mm] = pk2(t[0], t[1]); f[2 * mm + 1] = pk2(t[2], t[3]);
        }
        fr[hf] = v4u{f[0], f[1], f[2], f[3]};
      }
      const int lane = fresh_lane(), l16 = lane * 16, j = lane & 15, h = lane >> 4;
      LAS v4u* vx = (LAS v4u*)(L + LDS_VX + (w & 3) * 2 * KB + l16);
      if (w >= 4) { vx[0] = fr[0]; vx[64] = fr[1]; }
      __syncthreads();
      {
        f32x4 kv = zero, z = zero;
        if (w < 4) {
          kv = mfma32(fr[0], vx[0], kv); z = mfma32(fr[0], ones4, z);
          kv = mfma32(fr[1], vx[64], kv); z = mfma32(fr[1], ones4, z);
        }
        asm volatile("" : "+v"(kv), "+v"(z));
        const v4u kvu = __builtin_bit_cast(v4u, kv), zu = __builtin_bit_cast(v4u, z);
        st128c(rsk, v4u{san(kvu.x), san(kvu.y), san(kvu.z), san(kvu.w)}, l16, OFF_KVP + l * KVPBYTES + (g * H + hd) * KB);
        st128c(rsk, v4u{san(zu.x), san(zu.y), san(zu.z), san(zu.w)}, j == 0 ? h * 16 : OOBV, OFF_ZP + l * ZPBYTES + (g * H + hd) * 64);
      }
      SB();
      {
        const int hf = w >> 2;
        const LAS v4u* xl = (const LAS v4u*)(L + hf * LDS_REG + l16);
        const LAS float* st = (const LAS float*)(L + LDS_STAT + hf * 256);
        v4u xr[4][2];
#pragma unroll
        for (int u = 0; u < 4; ++u) { xr[u][0] = xl[(u * 2) * 64]; xr[u][1] = xl[(u * 2 + 1) * 64]; }
        SB();
        w1[0] = LD1N(0); w1[16] = LD1N(16); w1[1] = LD1N(1); w1[17] = LD1N(17);
        if (l == 0) {
          f32x4 cs = zero;
#pragma unroll
          for (int u = 0; u < 16; ++u) cs = mfma32(wqq[u], ones4, cs);
          if (j == 0) *(LAS f32x4*)(csl + 16 * (w & 3) + 4 * h) = cs;
        }
        f32x4 aq[2] = {zero, zero};
#pragma unroll
        for (int u = 0; u < 16; ++u) {
          aq[0] = mfma32(wqq[u], xr[u % 4][0], aq[0]);
          aq[1] = mfma32(wqq[u], xr[u % 4][1], aq[1]);
          if (u + 4 < 16) { xr[u % 4][0] = xl[((u + 4) * 2) * 64]; xr[u % 4][1] = xl[((u + 4) * 2 + 1) * 64]; }
          if (u < 12) { const int t = u >> 1; if ((u & 1) == 0) w1[2 + t] = LD1N(2 + t); else w1[18 + t] = LD1N(18 + t); }
          SB();
        }
        unsigned pq[4];
        const f32x4 csq = *(const LAS f32x4*)(csl + 16 * (w & 3) + 4 * h);
#pragma unroll
        for (int mm = 0; mm < 2; ++mm) {
          const float mean = st[16 * mm + j], rstd = st[32 + 16 * mm + j];
          float q[4];
#pragma unroll
          for (int r = 0; r < 4; ++r) q[r] = phi((aq[mm][r] - mean * csq[r]) * rstd);
          pq[2 * mm] = pk2s(q[0], q[1]); pq[2 * mm + 1] = pk2s(q[2], q[3]);
        }
        st128c(rs, v4u{pq[0], pq[1], pq[2], pq[3]}, l16, (l == 0 ? OFF_PQ0 : OFF_PQ1) + ((g * 2 + hf) * H + hd) * KB);
        SB();
      }
    }
    STAMP(1 + 6 * l);
    if (w == 4) {
      const int lane = fresh_lane();
      const int ch = lane & 7, gq = lane >> 3;
      v4u p[4]; unsigned n = 0;
      for (;;) {
#pragma unroll
        for (int i = 0; i < 4; ++i) p[i] = ld128c(rs, gq * H * KB + ch * 16, OFF_KVP + l * KVPBYTES + (i * 8 * H + g) * KB + c * 128);
        if (valid(umax(umax(umax4(p[0]), umax4(p[1])), umax(umax4(p[2]), umax4(p[3]))))) break;
        if (spin_fail(n, rs)) break;
      }
      f32x4 s = (__builtin_bit_cast(f32x4, p[0]) + __builtin_bit_cast(f32x4, p[1])) + (__builtin_bit_cast(f32x4, p[2]) + __builtin_bit_cast(f32x4, p[3]));
#pragma unroll
      for (int r = 0; r < 4; ++r) s[r] = sum_x32(sum_x16(ror_add<8>(s[r])));
      if (lane < 8) st64c(rs, v2u{pk2s(s.x, s.y), pk2s(s.z, s.w)}, lane * 16, OFF_KVR + l * KVRBYTES + ((g >> 1) * 64 + 8 * c) * 16 + (g & 1) * 8);
    } else if (w == 5 && c == 0) {
      const int lane = fresh_lane();
      const int hh = lane & 3, gq = lane >> 2;
      v4u p[2]; unsigned n = 0;
      for (;;) {
#pragma unroll
        for (int i = 0; i < 2; ++i) p[i] = ld128c(rs, gq * H * 64 + hh * 16, OFF_ZP + l * ZPBYTES + (i * 16 * H + g) * 64);
        if (valid(umax(umax4(p[0]), umax4(p[1])))) break;
        if (spin_fail(n, rs)) break;
      }
      f32x4 s = __builtin_bit_cast(f32x4, p[0]) + __builtin_bit_cast(f32x4, p[1]);
#pragma unroll
      for (int r = 0; r < 4; ++r) s[r] = sum_x32(sum_x16(ror_add<8>(ror_add<4>(s[r]))));
      if (lane < 4) st64c(rs, v2u{pk2s(s.x, s.y), pk2s(s.z, s.w)}, lane * 32, OFF_ZR + l * ZRBYTES + (g >> 2) * 128 + (g & 3) * 8);
    }
    {
      const int lane = fresh_lane(), l16 = lane * 16, h = lane >> 4;
      int sp_ = (l == 0 ? OFF_PQ0 : OFF_PQ1) + (g * 2 * H + 4 * w) * KB; OPQ(sp_);
      v4u pqa[2][4];
      __builtin_amdgcn_s_sleep(PQ_SLEEP);
#pragma unroll
      for (int hh = 0; hh < 4; ++hh) { pqa[0][hh] = ld128c(rs, l16 + hh * KB, sp_); pqa[1][hh] = ld128c(rs, l16 + hh * KB, sp_ + H * KB); }
      SB();
      if (w == 7) {
        v4u t[17];
        unsigned n = 0;
        for (;;) {
#pragma unroll
          for (int i = 0; i < 16; ++i) t[i] = ld128c(rs, l16 + (i & 3) * KB, OFF_KVR + l * KVRBYTES + (i >> 2) * 4 * KB);
          t[16] = ld128c(rs, l16, OFF_ZR + l * ZRBYTES);
          unsigned m = 0;
#pragma unroll
          for (int i = 0; i < 17; ++i) m = umax(m, umax4(t[i]));
          if (valid(m)) break;
          if (spin_fail(n, rs)) break;
        }
#pragma unroll
        for (int i = 0; i < 17; ++i) *(LAS v4u*)(L + LDS_KV + i * KB + l16) = t[i];
      }
      STAMP(2 + 6 * l);
      __syncthreads();
      v4u kvf[2], zf[2];
      kvf[0] = *(const LAS v4u*)(L + LDS_KV + 2 * w * KB + l16);
      kvf[1] = *(const LAS v4u*)(L + LDS_KV + (2 * w + 1) * KB + l16);
      zf[0] = *(const LAS v4u*)(L + LDS_KV + 16 * KB + w * 128 + h * 32);
      zf[1] = *(const LAS v4u*)(L + LDS_KV + 16 * KB + w * 128 + h * 32 + 16);
      const int x1o = OFF_X1 + 2 * l * XBYTES + g * 64 * KB, s1o = OFF_ST1 + 2 * l * STBYTES + g * 8 * KB;
#pragma unroll
      for (int hf = 0; hf < 2; ++hf) {
        v4u (&pq)[4] = pqa[hf];
        if (__builtin_expect(!valid(umax(umax(umax4(pq[0]), umax4(pq[1])), umax(umax4(pq[2]), umax4(pq[3])))), 0)) {
          unsigned n = 0;
          do {
            if (spin_fail(n, rs)) break;
#pragma unroll
            for (int hh = 0; hh < 4; ++hh) reld128(pq[hh], ws, l16 + hh * KB + sp_ + hf * H * KB);
            asm volatile("s_waitcnt vmcnt(0)" : "+v"(pq[0]), "+v"(pq[1]), "+v"(pq[2]), "+v"(pq[3]) :: "memory");
          } while (!valid(umax(umax(umax4(pq[0]), umax4(pq[1])), umax(umax4(pq[2]), umax4(pq[3])))));
        }
        v4u of[2][2];
#pragma unroll
        for (int hp = 0; hp < 2; ++hp) {
          f32x4 num[2][2], den[2][2];
#pragma unroll
          for (int t = 0; t < 2; ++t) {
            const v4u p = pq[hp * 2 + t];
            const v2u ka = t ? v2u{kvf[hp].z, kvf[hp].w} : v2u{kvf[hp].x, kvf[hp].y};
            const v2u za = t ? v2u{zf[hp].z, zf[hp].w} : v2u{zf[hp].x, zf[hp].y};
            num[t][0] = mfma16(ka, v2u{p.x, p.y}, zero); den[t][0] = mfma16(za, v2u{p.x, p.y}, zero);
            num[t][1] = mfma16(ka, v2u{p.z, p.w}, zero); den[t][1] = mfma16(za, v2u{p.z, p.w}, zero);
          }
          SB();
#pragma unroll
          for (int mm = 0; mm < 2; ++mm) {
            const float r0 = __builtin_amdgcn_rcpf(den[0][mm].x + 1e-6f), r1 = __builtin_amdgcn_rcpf(den[1][mm].x + 1e-6f);
            of[hp][mm] = v4u{pk2(num[0][mm].x * r0, num[0][mm].y * r0), pk2(num[0][mm].z * r0, num[0][mm].w * r0),
                             pk2(num[1][mm].x * r1, num[1][mm].y * r1), pk2(num[1][mm].z * r1, num[1][mm].w * r1)};
          }
          SB();
        }
        SB();
        if (X1_POS == 0 && hf == 1) x_issue(xs, rs, OFF_X1 + 2 * l * XBYTES + sx_, OFF_ST1 + 2 * l * STBYTES + ss_);
#pragma unroll
        for (int u = 8 + 4 * hf; u < 12 + 4 * hf; ++u) { w1[u] = LD1N(u); w1[16 + u] = LD1N(16 + u); }
        LAS f32x4* red = (LAS f32x4*)(L + hf * LDS_REG + w * 8 * KB + l16);
#pragma unroll
        for (int nt = 0; nt < 4; ++nt) {
#pragma unroll
          for (int mm = 0; mm < 2; ++mm) {
            f32x4 acc = mfma32(wo[nt * 2], of[0][mm], zero);
            acc = mfma32(wo[nt * 2 + 1], of[1][mm], acc);
            red[(nt * 2 + mm) * 64] = acc;
          }
        }
        __syncthreads();
        if (X1_POS == 1 && hf == 1) x_issue(xs, rs, OFF_X1 + 2 * l * XBYTES + sx_, OFF_ST1 + 2 * l * STBYTES + ss_);
        red_out<false>(rsk, rsk, L, hf, x1o + hf * 32 * KB, s1o + hf * 4 * KB, g, c, hf, w);
        if (X1_POS == 2 && hf == 1) x_issue(xs, rs, OFF_X1 + 2 * l * XBYTES + sx_, OFF_ST1 + 2 * l * STBYTES + ss_);
      }
    }
    STAMP(3 + 6 * l);
    v4u hfr[14];
    int sh_ = OFF_H + l * HBYTES + ((g * 2 * 56 + 7 * w) * 2) * KB; OPQ(sh_);
    {
      const int xo = OFF_X1 + 2 * l * XBYTES + sx_, so = OFF_ST1 + 2 * l * STBYTES + ss_;
      if (l == 0) {
        f32x4 ca = zero, cb = zero;
#pragma unroll
        for (int u = 0; u < 16; ++u) { ca = mfma32(w1[u], ones4, ca); cb = mfma32(w1[16 + u], ones4, cb); }
        const int ln = fresh_lane();
        if ((ln & 15) == 0) { *(LAS f32x4*)(csl + 192 + 32 * w + 4 * (ln >> 4)) = ca; *(LAS f32x4*)(csl + 192 + 32 * w + 16 + 4 * (ln >> 4)) = cb; }
      }
#pragma unroll
      for (int hf = 0; hf < 2; ++hf) {
        x_finish(xs, rs, ws, xo + hf * 32 * KB, so + hf * 4 * KB, L, hf, w);
        const int lane = fresh_lane(), l16 = lane * 16, j = lane & 15, h = lane >> 4;
        const LAS v4u* xl = (const LAS v4u*)(L + hf * LDS_REG + l16);
        const LAS float* st = (const LAS float*)(L + LDS_STAT + hf * 256);
        f32x4 a0[2] = {zero, zero}, a1[2] = {zero, zero};
        v4u xr[3][2];
#pragma unroll
        for (int u = 0; u < 3; ++u) { xr[u][0] = xl[(u * 2) * 64]; xr[u][1] = xl[(u * 2 + 1) * 64]; }
        SB();
#pragma unroll
        for (int u = 0; u < 16; ++u) {
          const v4u x0 = xr[u % 3][0], x1 = xr[u % 3][1];
          a0[0] = mfma32(w1[u], x0, a0[0]); a1[0] = mfma32(w1[16 + u], x0, a1[0]);
          a0[1] = mfma32(w1[u], x1, a0[1]); a1[1] = mfma32(w1[16 + u], x1, a1[1]);
          if (u + 3 < 16) { xr[u % 3][0] = xl[((u + 3) * 2) * 64]; xr[u % 3][1] = xl[((u + 3) * 2 + 1) * 64]; }
          if (hf == 0) { if ((u & 1) == 0 && W2_WO + (u >> 1) < W2_EARLY) w2[W2_WO + (u >> 1)] = LD2(W2_WO + (u >> 1)); }
          else { const int i0 = W2_EARLY + ((28 - W2_EARLY) * u) / 16, i1 = W2_EARLY + ((28 - W2_EARLY) * (u + 1)) / 16;
#pragma unroll
                 for (int i = i0; i < i1; ++i) w2[i] = LD2(i); }
          if (hf == 0 && u == XI_U) x_issue(xs, rs, xo + 32 * KB, so + 4 * KB);
          SB();
        }
        if (hf == 0 && XI_U == 16) x_issue(xs, rs, xo + 32 * KB, so + 4 * KB);
        if (hf == 1) {
#pragma unroll
          for (int i = 0; i < 14; ++i) hfr[i] = LDH(i);
        }
        if (FILL_MAIN && hf == 0) {
          const int nf = l == 0 ? WS_TOTAL : 0;
          fill_group(__builtin_amdgcn_make_buffer_rsrc((void*)ws, 0, nf, 0x00020000), __builtin_amdgcn_make_buffer_rsrc((void*)ws, 0, w < 7 ? nf : 0, 0x00020000),
                     __builtin_amdgcn_make_buffer_rsrc((void*)ws, 0, w < 4 ? nf : 0, 0x00020000), 1);
        }
        SB();
        {
          const f32x4 c0 = *(const LAS f32x4*)(csl + 192 + 32 * w + 4 * h), c1 = *(const LAS f32x4*)(csl + 192 + 32 * w + 16 + 4 * h);
#pragma unroll
          for (int mm = 0; mm < 2; ++mm) {
            const float mean = st[16 * mm + j], rstd = st[32 + 16 * mm + j];
            float t[8];
#pragma unroll
            for (int r = 0; r < 4; ++r) { t[r] = (a0[mm][r] - mean * c0[r]) * rstd; t[4 + r] = (a1[mm][r] - mean * c1[r]) * rstd; }
#pragma unroll
            for (int r = 0; r < 8; ++r) t[r] = t[r] * __builtin_amdgcn_rcpf(1.f + fexp(-t[r]));
            st128c(rs1, v4u{pk2s(t[0], t[1]), pk2s(t[2], t[3]), pk2s(t[4], t[5]), pk2s(t[6], t[7])}, l16,
                   OFF_H + l * HBYTES + (((g * 2 + hf) * 56 + 7 * c + w) * 2 + mm) * KB);
          }
        }
        SB();
      }
    }
    STAMP(4 + 6 * l);
    {
      const rsrc_t rsn = __builtin_amdgcn_make_buffer_rsrc((void*)ws, 0, l == 0 ? WS_TOTAL : 0, 0x00020000);
      const rsrc_t rsx = __builtin_amdgcn_make_buffer_rsrc((void*)ws, 0, (l == 0 && w < 4) ? WS_TOTAL : 0, 0x00020000);
      const rsrc_t rso = __builtin_amdgcn_make_buffer_rsrc((void*)out, 0, (l == 1 && w < 4) ? S * D * 2 : 0, 0x00020000);
      const int x2o = OFF_X1 + XBYTES + g * 64 * KB, s2o = OFF_ST1 + STBYTES + g * 8 * KB;
#pragma unroll
      for (int hf = 0; hf < 2; ++hf) {
        const int l16 = fresh_lane() * 16;
        if (__builtin_expect(!valid(hmax(hfr)), 0)) {
          unsigned n = 0;
          do {
            if (spin_fail(n, rs)) break;
#pragma unroll
            for (int i = 0; i < 14; ++i) reld128(hfr[i], ws, l16 + sh_ + (hf * 112 + i) * KB);
            asm volatile("s_waitcnt vmcnt(0)" : "+v"(hfr[0]), "+v"(hfr[1]), "+v"(hfr[2]), "+v"(hfr[3]), "+v"(hfr[4]), "+v"(hfr[5]), "+v"(hfr[6]),
                         "+v"(hfr[7]), "+v"(hfr[8]), "+v"(hfr[9]), "+v"(hfr[10]), "+v"(hfr[11]), "+v"(hfr[12]), "+v"(hfr[13]) :: "memory");
          } while (!valid(hmax(hfr)));
        }
        if (hf == 0) STAMP(5 + 6 * l);
        f32x4 acc[4][2];
#pragma unroll
        for (int nt = 0; nt < 4; ++nt) { acc[nt][0] = zero; acc[nt][1] = zero; }
#pragma unroll
        for (int u = 0; u < 7; ++u) {
#pragma unroll
          for (int nt = 0; nt < 4; ++nt) {
            acc[nt][0] = mfma32(w2[u * 4 + nt], hfr[u * 2], acc[nt][0]);
            acc[nt][1] = mfma32(w2[u * 4 + nt], hfr[u * 2 + 1], acc[nt][1]);
          }
          if (hf == 1) {
#pragma unroll
            for (int i = (WKV_LOOP * u) / 7; i < (WKV_LOOP * (u + 1)) / 7; ++i) wkv[i] = LDK(rsn, i);
          }
          SB();
        }
        if (hf == 0) {
        } else {
          x_issue(xs, rsn, OFF_X1 + XBYTES + sx_, OFF_ST1 + STBYTES + ss_);
        }
        red_write(acc, L, hf, w);
        __syncthreads();
        if (hf == 0) {
#pragma unroll
          for (int i = 0; i < 14; ++i) hfr[i] = LDH(112 + i);
        }
        red_out<true>(rsx, rso, L, hf, x2o + hf * 32 * KB, s2o + hf * 4 * KB, g, c, hf, w);
        if (hf == 1) {
#pragma unroll
          for (int i = WKV_LOOP; i < 16; ++i) wkv[i] = LDK(rsn, i);
        }
      }
    }
    STAMP(6 + 6 * l);
  }
  STAMP_FLUSH;
}
}

extern "C" void kernel_launch(void* const* d_in, const int* in_sizes, int n_in, void* d_out, int out_size, void* d_ws, size_t ws_size, hipStream_t stream) {
  const uint16_t* x = (const uint16_t*)d_in[0];
  const uint16_t* wqkv = (const uint16_t*)d_in[1];
  const uint16_t* wo = (const uint16_t*)d_in[2];
  const uint16_t* w1 = (const uint16_t*)d_in[4];
  const uint16_t* w2 = (const uint16_t*)d_in[5];
  unsigned char* ws = (unsigned char*)d_ws;
  uint16_t* out = (uint16_t*)d_out;
  (void)hipFuncSetAttribute((const void*)k_main, hipFuncAttributeMaxDynamicSharedMemorySize, LDS_BYTES);
  k_prep<<<PREP_BLOCKS, 256, 0, stream>>>(wqkv, wo, w1, w2, ws);
  k_main<<<256, 512, LDS_BYTES, stream>>>(ws, x, out);
}
```

```cpp
#include <hip/hip_runtime.h>
#include <hip/hip_bf16.h>
#include <stdint.h>

#define GAS __attribute__((address_space(1)))
#define LAS __attribute__((address_space(3)))
typedef unsigned v4u __attribute__((ext_vector_type(4)));
typedef unsigned v2u __attribute__((ext_vector_type(2)));
typedef float f32x4 __attribute__((ext_vector_type(4)));
typedef float f32x2 __attribute__((ext_vector_type(2)));
typedef __bf16 bf16x8 __attribute__((ext_vector_type(8)));
typedef __bf16 bf16x2 __attribute__((ext_vector_type(2)));
typedef short s16x4 __attribute__((ext_vector_type(4)));

#define STAMP(slot)
#define STAMP_DECL
#define STAMP_FLUSH

namespace {
constexpr int S = 2048, D = 512, F = 1792, H = 32;
constexpr int NG = 32;
constexpr int KB = 1024;
constexpr unsigned SENT = 0xFFFFFFFFu;
constexpr int OFF_WQKV = 0;
constexpr int OFF_WO = OFF_WQKV + 96 * 16 * KB;
constexpr int OFF_W1 = OFF_WO + 32 * 16 * KB;
constexpr int OFF_W2 = OFF_W1 + 112 * 16 * KB;
constexpr int OFF_CSQ = OFF_W2 + 32 * 56 * KB;
constexpr int OFF_CS1 = OFF_CSQ + 8 * KB;
constexpr int OFF_TMO = OFF_CS1 + 8 * KB;
constexpr int XBYTES = NG * 64 * KB;
constexpr int STBYTES = NG * 8 * KB;
constexpr int OFF_X0 = OFF_TMO + 4 * KB;
constexpr int OFF_ST0 = OFF_X0 + XBYTES;
constexpr int OFF_FILL0 = OFF_ST0 + STBYTES;
constexpr int KVPBYTES = NG * H * KB;
constexpr int OFF_KVP = OFF_FILL0;
constexpr int ZPBYTES = NG * H * 64;
constexpr int OFF_ZP = OFF_KVP + 2 * KVPBYTES;
constexpr int KVRBYTES = 16 * KB;
constexpr int OFF_KVR = OFF_ZP + 2 * ZPBYTES;
constexpr int ZRBYTES = KB;
constexpr int OFF_ZR = OFF_KVR + 2 * KVRBYTES;
constexpr int OFF_ST1 = OFF_ZR + 2 * ZRBYTES;
constexpr int OFF_PQ0 = OFF_ST1 + 3 * STBYTES;
constexpr int OFF_X1 = OFF_PQ0 + XBYTES;
#define FILL_MAIN 0
constexpr int OFF_PQ1 = OFF_X1 + 3 * XBYTES;
constexpr int HBYTES = NG * 224 * KB;
constexpr int OFF_H = OFF_PQ1 + XBYTES;
constexpr int WS_TOTAL = OFF_H + 2 * HBYTES;
constexpr int OFF_FILL1 = FILL_MAIN ? OFF_X1 + XBYTES : WS_TOTAL;
static_assert((OFF_FILL0 % 1024) == 0 && (OFF_FILL1 % 1024) == 0, "");
__host__ __device__ constexpr int xoff(int xi) { return xi == 0 ? OFF_X0 : OFF_X1 + (xi - 1) * XBYTES; }
__host__ __device__ constexpr int stoff(int xi) { return xi == 0 ? OFF_ST0 : OFF_ST1 + (xi - 1) * STBYTES; }

__device__ __forceinline__ f32x4 mfma32(v4u a, v4u b, f32x4 c) {
  return __builtin_amdgcn_mfma_f32_16x16x32_bf16(__builtin_bit_cast(bf16x8, a), __builtin_bit_cast(bf16x8, b), c, 0, 0, 0);
}
__device__ __forceinline__ f32x4 mfma16(v2u a, v2u b, f32x4 c) {
  return __builtin_amdgcn_mfma_f32_16x16x16bf16_1k(__builtin_bit_cast(s16x4, a), __builtin_bit_cast(s16x4, b), c, 0, 0, 0);
}
__device__ __forceinline__ unsigned pk2(float lo, float hi) {
  bf16x2 v; v.x = (__bf16)lo; v.y = (__bf16)hi;
  return __builtin_bit_cast(unsigned, v);
}
__device__ __forceinline__ unsigned san(unsigned u) { return u < 0xFFFFFFFEu ? u : 0xFFFFFFFEu; }
__device__ __forceinline__ unsigned pk2s(float lo, float hi) { return san(pk2(lo, hi)); }
__device__ __forceinline__ float asf(unsigned u) { return __builtin_bit_cast(float, u); }
__device__ __forceinline__ float bflo(unsigned u) { return __builtin_bit_cast(float, u << 16); }
__device__ __forceinline__ float bfhi(unsigned u) { return __builtin_bit_cast(float, u & 0xFFFF0000u); }
__device__ __forceinline__ float fexp(float t) { return __builtin_amdgcn_exp2f(t * 1.44269504088896f); }
__device__ __forceinline__ float phi(float t) { return fmaxf(t, 0.f) + fexp(fminf(t, 0.f)) + 1e-6f; }
__device__ __forceinline__ int permk(int h, int i) { return i < 4 ? 4 * h + i : 16 + 4 * h + (i - 4); }
#define XMAP 2
__device__ __forceinline__ void blk_gc(int b, int& g, int& c) {
  const int xcd = b & 7, idx = b >> 3;
  if (XMAP == 1) { g = xcd * 4 + (idx >> 3); c = idx & 7; }
  else if (XMAP == 2) { g = (xcd >> 1) * 8 + (idx >> 2); c = (xcd & 1) * 4 + (idx & 3); }
  else { g = (xcd >> 2) * 16 + (idx >> 1); c = (xcd & 3) * 2 + (idx & 1); }
}
__device__ __forceinline__ unsigned umax(unsigned a, unsigned b) { return a > b ? a : b; }
__device__ __forceinline__ unsigned umax4(v4u v) { return umax(umax(v.x, v.y), umax(v.z, v.w)); }
#define SB() __builtin_amdgcn_sched_barrier(0)

constexpr int PREP_BLOCKS = 352;
__global__ __launch_bounds__(256) void k_prep(const uint16_t* __restrict__ wqkv, const uint16_t* __restrict__ wo,
                                              const uint16_t* __restrict__ w1, const uint16_t* __restrict__ w2, unsigned char* __restrict__ ws) {
  __shared__ __attribute__((aligned(16))) unsigned char sm[4 * 32 * 144];
  const int tid = threadIdx.x, lane = tid & 63, w = __builtin_amdgcn_readfirstlane(tid >> 6);
  int id = blockIdx.x * 4 + w;
  const uint16_t* W; int N, off, UT, u, ng;
  if (id < 384) { W = wqkv; N = 1536; off = OFF_WQKV; UT = 16; u = id & 15; ng = id >> 4; }
  else if (id < 512) { id -= 384; W = wo; N = 512; off = OFF_WO; UT = 16; u = id & 15; ng = id >> 4; }
  else if (id < 960) { id -= 512; W = w1; N = 1792; off = OFF_W1; UT = 16; u = id & 15; ng = id >> 4; }
  else { id -= 960; W = w2; N = 512; off = OFF_W2; UT = 56; u = id % 56; ng = id / 56; }
  v4u v[4];
#pragma unroll
  for (int q = 0; q < 4; ++q) v[q] = *(const v4u*)(W + (size_t)(32 * u + 8 * q + (lane >> 3)) * N + ng * 64 + (lane & 7) * 8);
  {
    const int n16 = (OFF_FILL1 - OFF_FILL0) / 16;
    v4u* p = (v4u*)(ws + OFF_FILL0);
    for (int i = blockIdx.x * 256 + tid; i < n16; i += PREP_BLOCKS * 256) p[i] = v4u{SENT, SENT, SENT, SENT};
    if (blockIdx.x == 0 && tid == 0) *(unsigned*)(ws + OFF_TMO) = 0u;
  }
  LAS unsigned char* S = (LAS unsigned char*)sm + w * (32 * 144);
#pragma unroll
  for (int q = 0; q < 4; ++q) *(LAS v4u*)(S + (8 * q + (lane >> 3)) * 144 + (lane & 7) * 16) = v[q];
  const int h = lane >> 4, qq = (lane & 15) >> 2, p = lane & 3;
#pragma unroll
  for (int t = 0; t < 4; ++t) {
    const s16x4 lo = __builtin_amdgcn_ds_read_tr16_b64_v4i16((LAS s16x4*)(S + (4 * h + qq) * 144 + (16 * t + 4 * p) * 2));
    const s16x4 hi = __builtin_amdgcn_ds_read_tr16_b64_v4i16((LAS s16x4*)(S + (16 + 4 * h + qq) * 144 + (16 * t + 4 * p) * 2));
    const v2u a = __builtin_bit_cast(v2u, lo), b = __builtin_bit_cast(v2u, hi);
    *(v4u*)(ws + off + ((size_t)((ng * 4 + t) * UT + u) * 64 + lane) * 16) = v4u{a.x, a.y, b.x, b.y};
  }
}

typedef __amdgpu_buffer_rsrc_t rsrc_t;
constexpr int SC1 = 16;
__device__ __forceinline__ v4u ld128(rsrc_t rs, int voff, int soff) { return __builtin_amdgcn_raw_buffer_load_b128(rs, voff, soff, 0); }
__device__ __forceinline__ v4u ld128c(rsrc_t rs, int voff, int soff) { return __builtin_amdgcn_raw_buffer_load_b128(rs, voff, soff, SC1); }
__device__ __forceinline__ v2u ld64c(rsrc_t rs, int voff, int soff) { return __builtin_amdgcn_raw_buffer_load_b64(rs, voff, soff, SC1); }
__device__ __forceinline__ unsigned ld32c(rsrc_t rs, int voff, int soff) { return __builtin_amdgcn_raw_buffer_load_b32(rs, voff, soff, SC1); }
__device__ __forceinline__ void st128c(rsrc_t rs, v4u v, int voff, int soff) { __builtin_amdgcn_raw_buffer_store_b128(v, rs, (int)((unsigned)voff + (unsigned)soff), 0, SC1); }
__device__ __forceinline__ void st64c(rsrc_t rs, v2u v, int voff, int soff) { __builtin_amdgcn_raw_buffer_store_b64(v, rs, voff, soff, SC1); }
__device__ __forceinline__ void st32c(rsrc_t rs, unsigned v, int voff, int soff) { __builtin_amdgcn_raw_buffer_store_b32(v, rs, voff, soff, SC1); }
__device__ __forceinline__ bool valid(unsigned m) { return __builtin_amdgcn_ballot_w64(m == SENT) == 0ull; }
__device__ __forceinline__ unsigned asu(float f) { return __builtin_bit_cast(unsigned, f); }
template <int N> __device__ __forceinline__ float ror_add(float x) {
  return x + asf((unsigned)__builtin_amdgcn_update_dpp(0, (int)asu(x), 0x120 | N, 0xF, 0xF, false));
}
__device__ __forceinline__ float sum_x16(float x) { auto r = __builtin_amdgcn_permlane16_swap(asu(x), asu(x), false, false); return asf(r[0]) + asf(r[1]); }
__device__ __forceinline__ float sum_x32(float x) { auto r = __builtin_amdgcn_permlane32_swap(asu(x), asu(x), false, false); return asf(r[0]) + asf(r[1]); }
constexpr unsigned SPIN_CAP = 1u << 14;
__device__ __forceinline__ bool spin_fail(unsigned& n, rsrc_t rs) {
  asm volatile("" ::: "memory");
  __builtin_amdgcn_s_sleep(2);
  ++n;
  if (n > SPIN_CAP) { st32c(rs, 1u, OFF_TMO, 0); return true; }
  if ((n & 63u) == 0u) { if (__builtin_amdgcn_readfirstlane(ld32c(rs, OFF_TMO, 0)) != 0) return true; }
  return false;
}

#define DUTY_POS 0
#define W1_G 2
#define POLL_K0 2
#define PQ_K 3
#define WKV_LOOP 10
#define PQ_SLEEP 32
#define ROLE_ORD 1
#define W2X 6
#define WO_G1 1
#define W1_G1 0
#define PQ_POS 1
#define X1_POS 2
#define WQQ_POS 2
#define H1_POS 1
#define H1_U 3
#define W1_Q 6
#define XI_U 16
#define W1_EARLY 14
#define KVR_SLEEP 4
#define W1_MID 6
#define W2_EARLY 8
#define W2_WO 0
constexpr int LDS_REG = 64 * KB;
constexpr int LDS_STAT = 2 * LDS_REG;
constexpr int LDS_CS = LDS_STAT + 512;
constexpr int LDS_RES = LDS_CS + 2048;
constexpr int LDS_VX = LDS_RES + 8 * KB;
constexpr int LDS_KV = LDS_VX;
constexpr int LDS_BYTES = LDS_VX + 17 * KB;
constexpr int OOBV = 0x7FFF0000;

__device__ __forceinline__ int fresh_lane() {
  unsigned z;
  asm volatile("s_mov_b32 %0, 0" : "=s"(z));
  return (int)__builtin_amdgcn_mbcnt_hi(~0u, __builtin_amdgcn_mbcnt_lo(~0u, z));
}
#define OPQ(x) asm volatile("" : "+s"(x))

__device__ __forceinline__ unsigned hmax(const v4u (&a)[14]) {
  unsigned m = 0;
#pragma unroll
  for (int i = 0; i < 14; ++i) m = umax(m, umax4(a[i]));
  return m;
}
__device__ __forceinline__ void reld128(v4u& a, const unsigned char* base, unsigned off) {
  asm volatile("s_nop 4\n\tglobal_load_dwordx4 %0, %1, %2 sc1" : "+v"(a) : "v"(off), "s"(base) : "memory");
}
__device__ __forceinline__ void reld64(v2u& a, const unsigned char* base, unsigned off) {
  asm volatile("s_nop 4\n\tglobal_load_dwordx2 %0, %1, %2 sc1" : "+v"(a) : "v"(off), "s"(base) : "memory");
}
struct XS { v4u x[4]; v2u s; };
__device__ __forceinline__ void x_issue(XS& a, rsrc_t rs, int xo, int so) {
  const int ln = fresh_lane();
#pragma unroll
  for (int i = 0; i < 4; ++i) a.x[i] = ld128c(rs, ln * 16 + i * KB, xo);
  a.s = ld64c(rs, ln * 8, so);
}
__device__ __forceinline__ unsigned x_max(const XS& a) { return umax(umax(umax4(a.x[0]), umax4(a.x[1])), umax(umax(umax4(a.x[2]), umax4(a.x[3])), umax(a.s.x, a.s.y))); }
__device__ __forceinline__ void x_finish(XS& a, rsrc_t rs, const unsigned char* ws, int xo, int so, LAS unsigned char* L, int par, int w) {
  if (__builtin_expect(!valid(x_max(a)), 0)) {
    unsigned n = 0;
    do {
      if (spin_fail(n, rs)) break;
      const unsigned ln = (unsigned)fresh_lane();
#pragma unroll
      for (int i = 0; i < 4; ++i) reld128(a.x[i], ws, ln * 16 + xo + i * KB);
      reld64(a.s, ws, ln * 8 + so);
      asm volatile("s_waitcnt vmcnt(0)" : "+v"(a.x[0]), "+v"(a.x[1]), "+v"(a.x[2]), "+v"(a.x[3]), "+v"(a.s) :: "memory");
    } while (!valid(x_max(a)));
  }
  const int ln = fresh_lane();
  LAS unsigned char* dst = L + par * LDS_REG + w * 4096 + ln * 16;
#pragma unroll
  for (int i = 0; i < 4; ++i) *(LAS v4u*)(dst + i * KB) = a.x[i];
  const float mp = asf(a.s.x), m2p = asf(a.s.y);
  const float mean = ror_add<1>(ror_add<2>(ror_add<4>(ror_add<8>(mp)))) * (1.f / 16.f);
  const float dm = mp - mean;
  const float m2 = ror_add<1>(ror_add<2>(ror_add<4>(ror_add<8>(m2p + 32.f * dm * dm))));
  const float rstd = __builtin_amdgcn_rsqf(m2 * (1.f / 512.f) + 1e-5f);
  if ((ln & 15) == 0) {
    LAS float* st = (LAS float*)(L + LDS_STAT + par * 256) + w * 4 + (ln >> 4);
    st[0] = mean; st[32] = rstd;
  }
  __syncthreads();
}

__device__ __forceinline__ void xraw_issue(XS& a, rsrc_t rx, int row0) {
  const int ln = fresh_lane();
#pragma unroll
  for (int i = 0; i < 4; ++i) a.x[i] = ld128(rx, ln * 16 + i * KB, row0 * KB);
}
__device__ __forceinline__ float wave_sum(float x) { return sum_x32(sum_x16(ror_add<1>(ror_add<2>(ror_add<4>(ror_add<8>(x)))))); }
__device__ __forceinline__ void xraw_finish(XS& a, LAS unsigned char* L, int par, int w) {
  const int ln = fresh_lane();
  const int u = ln >> 2, q = ln & 3;
#pragma unroll
  for (int i = 0; i < 4; ++i) {
    const int tok = 4 * w + i;
    LAS unsigned char* d = L + par * LDS_REG + (u * 2 + (tok >> 4)) * KB + ((tok & 15) + 32 * (q & 1)) * 16 + (q >> 1) * 8;
    *(LAS v2u*)d = v2u{a.x[i].x, a.x[i].y};
    *(LAS v2u*)(d + 256) = v2u{a.x[i].z, a.x[i].w};
    const float v[8] = {bflo(a.x[i].x), bfhi(a.x[i].x), bflo(a.x[i].y), bfhi(a.x[i].y), bflo(a.x[i].z), bfhi(a.x[i].z), bflo(a.x[i].w), bfhi(a.x[i].w)};
    const float mean = wave_sum(((v[0] + v[1]) + (v[2] + v[3])) + ((v[4] + v[5]) + (v[6] + v[7]))) * (1.f / 512.f);
    float m2 = 0.f;
#pragma unroll
    for (int e = 0; e < 8; ++e) { const float dd = v[e] - mean; m2 += dd * dd; }
    m2 = wave_sum(m2);
    if (ln == 0) {
      LAS float* st = (LAS float*)(L + LDS_STAT + par * 256) + tok;
      st[0] = mean; st[32] = __builtin_amdgcn_rsqf(m2 * (1.f / 512.f) + 1e-5f);
    }
  }
  __syncthreads();
}

__device__ __forceinline__ void red_write(f32x4 (&acc)[4][2], LAS unsigned char* L, int par, int w) {
  const int ln = fresh_lane();
  LAS f32x4* red = (LAS f32x4*)(L + par * LDS_REG + w * 8 * KB + ln * 16);
#pragma unroll
  for (int nt = 0; nt < 4; ++nt)
#pragma unroll
    for (int mm = 0; mm < 2; ++mm) red[(nt * 2 + mm) * 64] = acc[nt][mm];
}
template <bool LAST>
__device__ __forceinline__ void red_out(rsrc_t rsw, rsrc_t rso, LAS unsigned char* L, int par, int xo, int so, int g, int c, int hf, int w) {
  const int ln = fresh_lane();
  const int mm = (w >> 1) & 1, uu = w & 1;
  const int j = ln & 15, h = ln >> 4;
  v4u o = {0, 0, 0, 0};
  v2u stv = {0, 0};
  if (w < 4) {
    const LAS f32x4* red = (const LAS f32x4*)(L + par * LDS_REG + (4 * uu + mm) * KB + ln * 16);
    LAS v4u* resp = (LAS v4u*)(L + LDS_RES + (hf * 4 + w) * KB + ln * 16);
    const v4u resid = *resp;
    f32x4 s0 = {0, 0, 0, 0}, s1 = {0, 0, 0, 0};
#pragma unroll
    for (int ww = 0; ww < 8; ++ww) {
      s0 += red[ww * 512];
      s1 += red[ww * 512 + 128];
    }
    s0.x += bflo(resid.x); s0.y += bfhi(resid.x); s0.z += bflo(resid.y); s0.w += bfhi(resid.y);
    s1.x += bflo(resid.z); s1.y += bfhi(resid.z); s1.z += bflo(resid.w); s1.w += bfhi(resid.w);
    o = v4u{pk2s(s0.x, s0.y), pk2s(s0.z, s0.w), pk2s(s1.x, s1.y), pk2s(s1.z, s1.w)};
    *resp = o;
  }
  asm volatile("" : "+v"(o));
  st128c(rsw, o, ln * 16, xo + ((2 * c + uu) * 2 + mm) * KB);
  if constexpr (LAST) {
    const int voff = j * (D * 2) + h * 8, soff = ((g * 64 + hf * 32 + 16 * mm) * D + 64 * c + 32 * uu) * 2;
    __builtin_amdgcn_raw_buffer_store_b64(v2u{o.x, o.y}, rso, voff, soff, 0);
    __builtin_amdgcn_raw_buffer_store_b64(v2u{o.z, o.w}, rso, voff, soff + 32, 0);
  }
  if (w < 4) {
    float v[8] = {bflo(o.x), bfhi(o.x), bflo(o.y), bfhi(o.y), bflo(o.z), bfhi(o.z), bflo(o.w), bfhi(o.w)};
    const float s = sum_x32(sum_x16(((v[0] + v[1]) + (v[2] + v[3])) + ((v[4] + v[5]) + (v[6] + v[7]))));
    const float mean = s * (1.f / 32.f);
    float m2 = 0.f;
#pragma unroll
    for (int i = 0; i < 8; ++i) { float d = v[i] - mean; m2 += d * d; }
    m2 = sum_x32(sum_x16(m2));
    stv = v2u{san(asu(mean)), san(asu(m2))};
  }
  asm volatile("" : "+v"(stv));
  st64c(rsw, stv, h == 0 ? j * 128 : OOBV, so + (16 * mm * 16 + 2 * c + uu) * 8);
}

template <int G>
__device__ __forceinline__ void run(unsigned char* ws, const uint16_t* xin, uint16_t* out, LAS unsigned char* L) {
  const int w = G * 4 + (__builtin_amdgcn_readfirstlane(threadIdx.x >> 6) & 3);
  int g, c; blk_gc(blockIdx.x, g, c);
  const rsrc_t rs = __builtin_amdgcn_make_buffer_rsrc((void*)ws, 0, WS_TOTAL, 0x00020000);
  const rsrc_t rx = __builtin_amdgcn_make_buffer_rsrc((void*)xin, 0, S * D * 2, 0x00020000);
  const rsrc_t rsnull = __builtin_amdgcn_make_buffer_rsrc((void*)ws, 0, 0, 0x00020000);
  const f32x4 zero = {0, 0, 0, 0};
  const int hd = 4 * c + (w & 3);
  int skv = OFF_WQKV + ((w < 4 ? 32 : 64) + hd) * 16 * KB;
  int sq_ = OFF_WQKV + hd * 16 * KB;
  int so_ = OFF_WO + (4 * c * 16 + 2 * w) * KB;
  int s1_ = OFF_W1 + (14 * c + 2 * (w < 7 ? w : 6)) * 16 * KB;
  int s2_ = OFF_W2 + (4 * c * 56 + 7 * w) * KB;
  int sx_ = w * 4096 + g * 64 * KB;
  int ss_ = w * 512 + g * 8 * KB;
  const rsrc_t rs1 = __builtin_amdgcn_make_buffer_rsrc((void*)ws, 0, w < 7 ? WS_TOTAL : 0, 0x00020000);
  const rsrc_t rsk = __builtin_amdgcn_make_buffer_rsrc((void*)ws, 0, w < 4 ? WS_TOTAL : 0, 0x00020000);
  STAMP_DECL
  STAMP(0);
#define LDO(i) ld128(rs, l16 + ((i) & 1) * KB, so_ + ((i) >> 1) * 16 * KB)
#define LD1(i) ld128(rs, l16 + ((i) & 3) * KB, s1_ + ((i) >> 2) * 4 * KB)
#define LD1N(i) ld128(rs1, l16 + ((i) & 3) * KB, s1_ + ((i) >> 2) * 4 * KB)
#define LD2(i) ld128(rs, l16 + (((i) >> 2) & 3) * KB, s2_ + (((i) & 3) * 56 + (((i) >> 2) & 4)) * KB)
#define LDK(r_, i) ld128(r_, l16 + ((i) & 3) * KB, skv + ((i) >> 2) * 4 * KB)
#define LDH(i) ld128c(rs, l16 + ((i) & 3) * KB, sh_ + ((i) >> 2) * 4 * KB)
  v4u wkv[16];
  XS xs;
  {
    const int lane = fresh_lane(), l16 = lane * 16;
    xraw_issue(xs, rx, g * 64 + 4 * w);
#pragma unroll
    for (int i = 0; i < 16; ++i) wkv[i] = LDK(rs, i);
    if (w < 4) {
#pragma unroll
      for (int hf = 0; hf < 2; ++hf) {
        const int vo_ = (lane & 15) * KB + (lane >> 4) * 8, so2 = (g * 64 + hf * 32 + 16 * (w >> 1)) * KB + (64 * c + 32 * (w & 1)) * 2;
        const v2u a = __builtin_amdgcn_raw_buffer_load_b64(rx, vo_, so2, 0), b = __builtin_amdgcn_raw_buffer_load_b64(rx, vo_ + 32, so2, 0);
        *(LAS v4u*)(L + LDS_RES + (hf * 4 + w) * KB + l16) = v4u{a.x, a.y, b.x, b.y};
      }
    }
  }
  LAS float* csl = (LAS float*)(L + LDS_CS);
  const v4u ones4 = {0x3F803F80u, 0x3F803F80u, 0x3F803F80u, 0x3F803F80u};
  auto fill_group = [&](rsrc_t rf, rsrc_t rf7, rsrc_t rf4, int layer) {
    const int l16 = fresh_lane() * 16;
    const v4u sv = {SENT, SENT, SENT, SENT};
#pragma unroll
    for (int i = 0; i < 4; ++i) st128c(rf7, sv, l16, OFF_H + layer * HBYTES + (((g * 2 + (i >> 1)) * 56 + 7 * c + w) * 2 + (i & 1)) * KB);
#pragma unroll
    for (int hf = 0; hf < 2; ++hf) st128c(rf4, sv, l16, OFF_X1 + (1 + layer) * XBYTES + (g * 2 + hf) * 32 * KB + ((2 * c + (w & 1)) * 2 + ((w >> 1) & 1)) * KB);
    if (layer == 0) st128c(rf, sv, l16, OFF_PQ1 + ((g * 2 + (w >> 2)) * H + hd) * KB);
  };
  SB();
#pragma unroll 1
  for (int l = 0; l < 2; ++l) {
    v4u wqq[16], wo[8], w1[32], w2[28];
    OPQ(skv); OPQ(sq_); OPQ(so_); OPQ(s1_); OPQ(s2_); OPQ(sx_); OPQ(ss_);
    {
      const int xo = OFF_X1 + XBYTES + sx_, so = OFF_ST1 + STBYTES + ss_;
      v4u fr[2];
#pragma unroll
      for (int hf = 0; hf < 2; ++hf) {
        if (l == 0) {
          if (hf == 0) {
            f32x4 cs = zero;
#pragma unroll
            for (int u = 0; u < 16; ++u) cs = mfma32(ones4, wkv[u], cs);
            const int ln = fresh_lane();
            if (ln < 16) csl[64 + 16 * w + ln] = cs.x;
          }
          xraw_finish(xs, L, hf, w);
        } else x_finish(xs, rs, ws, xo + hf * 32 * KB, so + hf * 4 * KB, L, hf, w);
        const int lane = fresh_lane(), l16 = lane * 16, j = lane & 15, h = lane >> 4;
        const LAS v4u* xl = (const LAS v4u*)(L + hf * LDS_REG + l16);
        const LAS float* st = (const LAS float*)(L + LDS_STAT + hf * 256);
        v4u xr[4][2];
#pragma unroll
        for (int u = 0; u < 4; ++u) { xr[u][0] = xl[(u * 2) * 64]; xr[u][1] = xl[(u * 2 + 1) * 64]; }
        if (ROLE_ORD && G == 1) {
#pragma unroll
          for (int i = 0; i < 8; ++i) wqq[hf * 8 + i] = ld128(rs, l16 + (i & 3) * KB, sq_ + (hf * 8 + (i & 4)) * KB);
          if (WO_G1 == hf) {
#pragma unroll
            for (int i = 0; i < 8; ++i) wo[i] = LDO(i);
          }
        }
        SB();
        f32x4 a[2] = {zero, zero};
#pragma unroll
        for (int u = 0; u < 16; ++u) {
          a[0] = mfma32(xr[u % 4][0], wkv[u], a[0]);
          a[1] = mfma32(xr[u % 4][1], wkv[u], a[1]);
          if (u + 4 < 16) { xr[u % 4][0] = xl[((u + 4) * 2) * 64]; xr[u % 4][1] = xl[((u + 4) * 2 + 1) * 64]; }
          if (hf == 0 && u == XI_U) { if (l == 0) xraw_issue(xs, rx, g * 64 + 32 + 4 * w); else x_issue(xs, rs, xo + 32 * KB, so + 4 * KB); }
          if (WQQ_POS == 1 && (u & 1) == 1) wqq[hf * 8 + (u >> 1)] = ld128(rs, l16 + ((u >> 1) & 3) * KB, sq_ + (hf * 8 + ((u >> 1) & 4)) * KB);
          SB();
        }
        if (hf == 0 && XI_U == 16) { if (l == 0) xraw_issue(xs, rx, g * 64 + 32 + 4 * w); else x_issue(xs, rs, xo + 32 * KB, so + 4 * KB); }
        if (WQQ_POS == 2 && !(ROLE_ORD && G == 1)) {
#pragma unroll
          for (int i = 0; i < 8; ++i) wqq[hf * 8 + i] = ld128(rs, l16 + (i & 3) * KB, sq_ + (hf * 8 + (i & 4)) * KB);
        }
        if (hf == 1 && !(ROLE_ORD && G == 1)) {
#pragma unroll
          for (int i = 0; i < 8; ++i) wo[i] = LDO(i);
        }
        if (FILL_MAIN && hf == 0) {
          const int nf = l == 0 ? WS_TOTAL : 0;
          fill_group(__builtin_amdgcn_make_buffer_rsrc((void*)ws, 0, nf, 0x00020000), __builtin_amdgcn_make_buffer_rsrc((void*)ws, 0, w < 7 ? nf : 0, 0x00020000),
                     __builtin_amdgcn_make_buffer_rsrc((void*)ws, 0, w < 4 ? nf : 0, 0x00020000), 0);
        }
        const float cs = csl[64 + 16 * w + j];
        unsigned f[4];
#pragma unroll
        for (int mm = 0; mm < 2; ++mm) {
          const f32x4 mean = *(const LAS f32x4*)(st + 16 * mm + 4 * h), rstd = *(const LAS f32x4*)(st + 32 + 16 * mm + 4 * h);
          float t[4];
#pragma unroll
          for (int r = 0; r < 4; ++r) { const float tt = (a[mm][r] - mean[r] * cs) * rstd[r]; t[r] = w < 4 ? phi(tt) : tt; }
          f[2 * mm] = pk2(t[0], t[1]); f[2 * mm + 1] = pk2(t[2], t[3]);
        }
        fr[hf] = v4u{f[0], f[1], f[2], f[3]};
      }
      const int lane = fresh_lane(), l16 = lane * 16, j = lane & 15, h = lane >> 4;
      LAS v4u* vx = (LAS v4u*)(L + LDS_VX + (w & 3) * 2 * KB + l16);
      if (w >= 4) { vx[0] = fr[0]; vx[64] = fr[1]; }
      __syncthreads();
      {
        f32x4 kv = zero, z = zero;
        if (w < 4) {
          kv = mfma32(fr[0], vx[0], kv); z = mfma32(fr[0], ones4, z);
          kv = mfma32(fr[1], vx[64], kv); z = mfma32(fr[1], ones4, z);
        }
        asm volatile("" : "+v"(kv), "+v"(z));
        const v4u kvu = __builtin_bit_cast(v4u, kv), zu = __builtin_bit_cast(v4u, z);
        st128c(rsk, v4u{san(kvu.x), san(kvu.y), san(kvu.z), san(kvu.w)}, l16, OFF_KVP + l * KVPBYTES + (g * H + hd) * KB);
        st128c(rsk, v4u{san(zu.x), san(zu.y), san(zu.z), san(zu.w)}, j == 0 ? h * 16 : OOBV, OFF_ZP + l * ZPBYTES + (g * H + hd) * 64);
      }
      SB();
      {
        const int hf = w >> 2;
        const LAS v4u* xl = (const LAS v4u*)(L + hf * LDS_REG + l16);
        const LAS float* st = (const LAS float*)(L + LDS_STAT + hf * 256);
        v4u xr[4][2];
#pragma unroll
        for (int u = 0; u < 4; ++u) { xr[u][0] = xl[(u * 2) * 64]; xr[u][1] = xl[(u * 2 + 1) * 64]; }
        SB();
        if (ROLE_ORD && G == 1 && WO_G1 == 2) {
#pragma unroll
          for (int i = 0; i < 8; ++i) wo[i] = LDO(i);
        }
        w1[0] = LD1N(0); w1[16] = LD1N(16); w1[1] = LD1N(1); w1[17] = LD1N(17);
        if (l == 0) {
          f32x4 cs = zero;
#pragma unroll
          for (int u = 0; u < 16; ++u) cs = mfma32(wqq[u], ones4, cs);
          if (j == 0) *(LAS f32x4*)(csl + 16 * (w & 3) + 4 * h) = cs;
        }
        f32x4 aq[2] = {zero, zero};
#pragma unroll
        for (int u = 0; u < 16; ++u) {
          aq[0] = mfma32(wqq[u], xr[u % 4][0], aq[0]);
          aq[1] = mfma32(wqq[u], xr[u % 4][1], aq[1]);
          if (u + 4 < 16) { xr[u % 4][0] = xl[((u + 4) * 2) * 64]; xr[u % 4][1] = xl[((u + 4) * 2 + 1) * 64]; }
          if (u < 12) { const int t = u >> 1; if ((u & 1) == 0) w1[2 + t] = LD1N(2 + t); else w1[18 + t] = LD1N(18 + t); }
          SB();
        }
        unsigned pq[4];
        const f32x4 csq = *(const LAS f32x4*)(csl + 16 * (w & 3) + 4 * h);
#pragma unroll
        for (int mm = 0; mm < 2; ++mm) {
          const float mean = st[16 * mm + j], rstd = st[32 + 16 * mm + j];
          float q[4];
#pragma unroll
          for (int r = 0; r < 4; ++r) q[r] = phi((aq[mm][r] - mean * csq[r]) * rstd);
          pq[2 * mm] = pk2s(q[0], q[1]); pq[2 * mm + 1] = pk2s(q[2], q[3]);
        }
        st128c(rs, v4u{pq[0], pq[1], pq[2], pq[3]}, l16, (l == 0 ? OFF_PQ0 : OFF_PQ1) + ((g * 2 + hf) * H + hd) * KB);
        SB();
      }
    }
    STAMP(1 + 6 * l);
    if (w == 4) {
      const int lane = fresh_lane();
      const int ch = lane & 7, gq = lane >> 3;
      v4u p[4]; unsigned n = 0;
      for (;;) {
#pragma unroll
        for (int i = 0; i < 4; ++i) p[i] = ld128c(rs, gq * H * KB + ch * 16, OFF_KVP + l * KVPBYTES + (i * 8 * H + g) * KB + c * 128);
        if (valid(umax(umax(umax4(p[0]), umax4(p[1])), umax(umax4(p[2]), umax4(p[3]))))) break;
        if (spin_fail(n, rs)) break;
      }
      f32x4 s = (__builtin_bit_cast(f32x4, p[0]) + __builtin_bit_cast(f32x4, p[1])) + (__builtin_bit_cast(f32x4, p[2]) + __builtin_bit_cast(f32x4, p[3]));
#pragma unroll
      for (int r = 0; r < 4; ++r) s[r] = sum_x32(sum_x16(ror_add<8>(s[r])));
      if (lane < 8) st64c(rs, v2u{pk2s(s.x, s.y), pk2s(s.z, s.w)}, lane * 16, OFF_KVR + l * KVRBYTES + ((g >> 1) * 64 + 8 * c) * 16 + (g & 1) * 8);
    } else if (w == 5 && c == 0) {
      const int lane = fresh_lane();
      const int hh = lane & 3, gq = lane >> 2;
      v4u p[2]; unsigned n = 0;
      for (;;) {
#pragma unroll
        for (int i = 0; i < 2; ++i) p[i] = ld128c(rs, gq * H * 64 + hh * 16, OFF_ZP + l * ZPBYTES + (i * 16 * H + g) * 64);
        if (valid(umax(umax4(p[0]), umax4(p[1])))) break;
        if (spin_fail(n, rs)) break;
      }
      f32x4 s = __builtin_bit_cast(f32x4, p[0]) + __builtin_bit_cast(f32x4, p[1]);
#pragma unroll
      for (int r = 0; r < 4; ++r) s[r] = sum_x32(sum_x16(ror_add<8>(ror_add<4>(s[r]))));
      if (lane < 4) st64c(rs, v2u{pk2s(s.x, s.y), pk2s(s.z, s.w)}, lane * 32, OFF_ZR + l * ZRBYTES + (g >> 2) * 128 + (g & 3) * 8);
    }
    {
      const int lane = fresh_lane(), l16 = lane * 16, h = lane >> 4;
      int sp_ = (l == 0 ? OFF_PQ0 : OFF_PQ1) + (g * 2 * H + 4 * w) * KB; OPQ(sp_);
      v4u pqa[2][4];
      __builtin_amdgcn_s_sleep(PQ_SLEEP);
#pragma unroll
      for (int hh = 0; hh < 4; ++hh) { pqa[0][hh] = ld128c(rs, l16 + hh * KB, sp_); pqa[1][hh] = ld128c(rs, l16 + hh * KB, sp_ + H * KB); }
      SB();
      if (w == 7) {
        v4u t[17];
        unsigned n = 0;
        for (;;) {
#pragma unroll
          for (int i = 0; i < 16; ++i) t[i] = ld128c(rs, l16 + (i & 3) * KB, OFF_KVR + l * KVRBYTES + (i >> 2) * 4 * KB);
          t[16] = ld128c(rs, l16, OFF_ZR + l * ZRBYTES);
          unsigned m = 0;
#pragma unroll
          for (int i = 0; i < 17; ++i) m = umax(m, umax4(t[i]));
          if (valid(m)) break;
          if (spin_fail(n, rs)) break;
        }
#pragma unroll
        for (int i = 0; i < 17; ++i) *(LAS v4u*)(L + LDS_KV + i * KB + l16) = t[i];
      }
      STAMP(2 + 6 * l);
      __syncthreads();
      v4u kvf[2], zf[2];
      kvf[0] = *(const LAS v4u*)(L + LDS_KV + 2 * w * KB + l16);
      kvf[1] = *(const LAS v4u*)(L + LDS_KV + (2 * w + 1) * KB + l16);
      zf[0] = *(const LAS v4u*)(L + LDS_KV + 16 * KB + w * 128 + h * 32);
      zf[1] = *(const LAS v4u*)(L + LDS_KV + 16 * KB + w * 128 + h * 32 + 16);
      const int x1o = OFF_X1 + 2 * l * XBYTES + g * 64 * KB, s1o = OFF_ST1 + 2 * l * STBYTES + g * 8 * KB;
#pragma unroll
      for (int hf = 0; hf < 2; ++hf) {
        v4u (&pq)[4] = pqa[hf];
        if (__builtin_expect(!valid(umax(umax(umax4(pq[0]), umax4(pq[1])), umax(umax4(pq[2]), umax4(pq[3])))), 0)) {
          unsigned n = 0;
          do {
            if (spin_fail(n, rs)) break;
#pragma unroll
            for (int hh = 0; hh < 4; ++hh) reld128(pq[hh], ws, l16 + hh * KB + sp_ + hf * H * KB);
            asm volatile("s_waitcnt vmcnt(0)" : "+v"(pq[0]), "+v"(pq[1]), "+v"(pq[2]), "+v"(pq[3]) :: "memory");
          } while (!valid(umax(umax(umax4(pq[0]), umax4(pq[1])), umax(umax4(pq[2]), umax4(pq[3])))));
        }
        v4u of[2][2];
#pragma unroll
        for (int hp = 0; hp < 2; ++hp) {
          f32x4 num[2][2], den[2][2];
#pragma unroll
          for (int t = 0; t < 2; ++t) {
            const v4u p = pq[hp * 2 + t];
            const v2u ka = t ? v2u{kvf[hp].z, kvf[hp].w} : v2u{kvf[hp].x, kvf[hp].y};
            const v2u za = t ? v2u{zf[hp].z, zf[hp].w} : v2u{zf[hp].x, zf[hp].y};
            num[t][0] = mfma16(ka, v2u{p.x, p.y}, zero); den[t][0] = mfma16(za, v2u{p.x, p.y}, zero);
            num[t][1] = mfma16(ka, v2u{p.z, p.w}, zero); den[t][1] = mfma16(za, v2u{p.z, p.w}, zero);
          }
          SB();
#pragma unroll
          for (int mm = 0; mm < 2; ++mm) {
            const float r0 = __builtin_amdgcn_rcpf(den[0][mm].x + 1e-6f), r1 = __builtin_amdgcn_rcpf(den[1][mm].x + 1e-6f);
            of[hp][mm] = v4u{pk2(num[0][mm].x * r0, num[0][mm].y * r0), pk2(num[0][mm].z * r0, num[0][mm].w * r0),
                             pk2(num[1][mm].x * r1, num[1][mm].y * r1), pk2(num[1][mm].z * r1, num[1][mm].w * r1)};
          }
          SB();
        }
        SB();
        if (X1_POS == 0 && hf == 1) x_issue(xs, rs, OFF_X1 + 2 * l * XBYTES + sx_, OFF_ST1 + 2 * l * STBYTES + ss_);
        if (ROLE_ORD && G == 1 && W1_G1) {
          if (hf == 1) {
#pragma unroll
            for (int u = 8; u < 16; ++u) { w1[u] = LD1N(u); w1[16 + u] = LD1N(16 + u); }
          }
        } else {
#pragma unroll
          for (int u = 8 + 4 * hf; u < 12 + 4 * hf; ++u) { w1[u] = LD1N(u); w1[16 + u] = LD1N(16 + u); }
        }
        LAS f32x4* red = (LAS f32x4*)(L + hf * LDS_REG + w * 8 * KB + l16);
#pragma unroll
        for (int nt = 0; nt < 4; ++nt) {
#pragma unroll
          for (int mm = 0; mm < 2; ++mm) {
            f32x4 acc = mfma32(wo[nt * 2], of[0][mm], zero);
            acc = mfma32(wo[nt * 2 + 1], of[1][mm], acc);
            red[(nt * 2 + mm) * 64] = acc;
          }
        }
        __syncthreads();
        if (X1_POS == 1 && hf == 1) x_issue(xs, rs, OFF_X1 + 2 * l * XBYTES + sx_, OFF_ST1 + 2 * l * STBYTES + ss_);
        red_out<false>(rsk, rsk, L, hf, x1o + hf * 32 * KB, s1o + hf * 4 * KB, g, c, hf, w);
        if (X1_POS == 2 && hf == 1) x_issue(xs, rs, OFF_X1 + 2 * l * XBYTES + sx_, OFF_ST1 + 2 * l * STBYTES + ss_);
      }
    }
    STAMP(3 + 6 * l);
    v4u hfr[14];
    int sh_ = OFF_H + l * HBYTES + ((g * 2 * 56 + 7 * w) * 2) * KB; OPQ(sh_);
    {
      const int xo = OFF_X1 + 2 * l * XBYTES + sx_, so = OFF_ST1 + 2 * l * STBYTES + ss_;
      if (l == 0) {
        f32x4 ca = zero, cb = zero;
#pragma unroll
        for (int u = 0; u < 16; ++u) { ca = mfma32(w1[u], ones4, ca); cb = mfma32(w1[16 + u], ones4, cb); }
        const int ln = fresh_lane();
        if ((ln & 15) == 0) { *(LAS f32x4*)(csl + 192 + 32 * w + 4 * (ln >> 4)) = ca; *(LAS f32x4*)(csl + 192 + 32 * w + 16 + 4 * (ln >> 4)) = cb; }
      }
#pragma unroll
      for (int hf = 0; hf < 2; ++hf) {
        x_finish(xs, rs, ws, xo + hf * 32 * KB, so + hf * 4 * KB, L, hf, w);
        const int lane = fresh_lane(), l16 = lane * 16, j = lane & 15, h = lane >> 4;
        const LAS v4u* xl = (const LAS v4u*)(L + hf * LDS_REG + l16);
        const LAS float* st = (const LAS float*)(L + LDS_STAT + hf * 256);
        f32x4 a0[2] = {zero, zero}, a1[2] = {zero, zero};
        v4u xr[3][2];
#pragma unroll
        for (int u = 0; u < 3; ++u) { xr[u][0] = xl[(u * 2) * 64]; xr[u][1] = xl[(u * 2 + 1) * 64]; }
        constexpr int W2S = W2_EARLY + ((ROLE_ORD && G == 1) ? W2X : 0);
        if (ROLE_ORD && G == 1 && hf == 0) {
#pragma unroll
          for (int i = 0; i < W2_EARLY; ++i) w2[i] = LD2(i);
        }
        if (ROLE_ORD && G == 1 && hf == 1) {
#pragma unroll
          for (int i = W2_EARLY; i < W2S; ++i) w2[i] = LD2(i);
        }
        SB();
#pragma unroll
        for (int u = 0; u < 16; ++u) {
          const v4u x0 = xr[u % 3][0], x1 = xr[u % 3][1];
          a0[0] = mfma32(w1[u], x0, a0[0]); a1[0] = mfma32(w1[16 + u], x0, a1[0]);
          a0[1] = mfma32(w1[u], x1, a0[1]); a1[1] = mfma32(w1[16 + u], x1, a1[1]);
          if (u + 3 < 16) { xr[u % 3][0] = xl[((u + 3) * 2) * 64]; xr[u % 3][1] = xl[((u + 3) * 2 + 1) * 64]; }
          if (hf == 0) { if (!ROLE_ORD && (u & 1) == 0 && W2_WO + (u >> 1) < W2_EARLY) w2[W2_WO + (u >> 1)] = LD2(W2_WO + (u >> 1)); }
          else { const int i0 = W2S + ((28 - W2S) * u) / 16, i1 = W2S + ((28 - W2S) * (u + 1)) / 16;
#pragma unroll
                 for (int i = i0; i < i1; ++i) w2[i] = LD2(i); }
          if (hf == 0 && u == XI_U) x_issue(xs, rs, xo + 32 * KB, so + 4 * KB);
          SB();
        }
        if (hf == 0 && XI_U == 16) x_issue(xs, rs, xo + 32 * KB, so + 4 * KB);
        if (ROLE_ORD && G == 0 && hf == 0) {
#pragma unroll
          for (int i = 0; i < W2_EARLY; ++i) w2[i] = LD2(i);
        }
        if (hf == 1) {
#pragma unroll
          for (int i = 0; i < 14; ++i) hfr[i] = LDH(i);
        }
        if (FILL_MAIN && hf == 0) {
          const int nf = l == 0 ? WS_TOTAL : 0;
          fill_group(__builtin_amdgcn_make_buffer_rsrc((void*)ws, 0, nf, 0x00020000), __builtin_amdgcn_make_buffer_rsrc((void*)ws, 0, w < 7 ? nf : 0, 0x00020000),
                     __builtin_amdgcn_make_buffer_rsrc((void*)ws, 0, w < 4 ? nf : 0, 0x00020000), 1);
        }
        SB();
        {
          const f32x4 c0 = *(const LAS f32x4*)(csl + 192 + 32 * w + 4 * h), c1 = *(const LAS f32x4*)(csl + 192 + 32 * w + 16 + 4 * h);
#pragma unroll
          for (int mm = 0; mm < 2; ++mm) {
            const float mean = st[16 * mm + j], rstd = st[32 + 16 * mm + j];
            float t[8];
#pragma unroll
            for (int r = 0; r < 4; ++r) { t[r] = (a0[mm][r] - mean * c0[r]) * rstd; t[4 + r] = (a1[mm][r] - mean * c1[r]) * rstd; }
#pragma unroll
            for (int r = 0; r < 8; ++r) t[r] = t[r] * __builtin_amdgcn_rcpf(1.f + fexp(-t[r]));
            st128c(rs1, v4u{pk2s(t[0], t[1]), pk2s(t[2], t[3]), pk2s(t[4], t[5]), pk2s(t[6], t[7])}, l16,
                   OFF_H + l * HBYTES + (((g * 2 + hf) * 56 + 7 * c + w) * 2 + mm) * KB);
          }
        }
        SB();
      }
    }
    STAMP(4 + 6 * l);
    {
      const rsrc_t rsn = __builtin_amdgcn_make_buffer_rsrc((void*)ws, 0, l == 0 ? WS_TOTAL : 0, 0x00020000);
      const rsrc_t rsx = __builtin_amdgcn_make_buffer_rsrc((void*)ws, 0, (l == 0 && w < 4) ? WS_TOTAL : 0, 0x00020000);
      const rsrc_t rso = __builtin_amdgcn_make_buffer_rsrc((void*)out, 0, (l == 1 && w < 4) ? S * D * 2 : 0, 0x00020000);
      const int x2o = OFF_X1 + XBYTES + g * 64 * KB, s2o = OFF_ST1 + STBYTES + g * 8 * KB;
#pragma unroll
      for (int hf = 0; hf < 2; ++hf) {
        const int l16 = fresh_lane() * 16;
        if (__builtin_expect(!valid(hmax(hfr)), 0)) {
          unsigned n = 0;
          do {
            if (spin_fail(n, rs)) break;
            unsigned vb = (unsigned)(fresh_lane() * 16 + sh_ + hf * 112 * KB);
#pragma unroll
            for (int i = 0; i < 14; ++i) { reld128(hfr[i], ws, vb); vb += KB; asm volatile("" : "+v"(vb)); }
            asm volatile("s_waitcnt vmcnt(0)" : "+v"(hfr[0]), "+v"(hfr[1]), "+v"(hfr[2]), "+v"(hfr[3]), "+v"(hfr[4]), "+v"(hfr[5]), "+v"(hfr[6]),
                         "+v"(hfr[7]), "+v"(hfr[8]), "+v"(hfr[9]), "+v"(hfr[10]), "+v"(hfr[11]), "+v"(hfr[12]), "+v"(hfr[13]) :: "memory");
          } while (!valid(hmax(hfr)));
        }
        if (hf == 0) STAMP(5 + 6 * l);
        f32x4 acc[4][2];
#pragma unroll
        for (int nt = 0; nt < 4; ++nt) { acc[nt][0] = zero; acc[nt][1] = zero; }
#pragma unroll
        for (int u = 0; u < 7; ++u) {
#pragma unroll
          for (int nt = 0; nt < 4; ++nt) {
            acc[nt][0] = mfma32(w2[u * 4 + nt], hfr[u * 2], acc[nt][0]);
            acc[nt][1] = mfma32(w2[u * 4 + nt], hfr[u * 2 + 1], acc[nt][1]);
          }
          if (hf == 1) {
#pragma unroll
            for (int i = (WKV_LOOP * u) / 7; i < (WKV_LOOP * (u + 1)) / 7; ++i) wkv[i] = LDK(rsn, i);
          }
          SB();
        }
        if (hf == 0) {
        } else {
          x_issue(xs, rsn, OFF_X1 + XBYTES + sx_, OFF_ST1 + STBYTES + ss_);
        }
        red_write(acc, L, hf, w);
        __syncthreads();
        if (hf == 0) {
#pragma unroll
          for (int i = 0; i < 14; ++i) hfr[i] = LDH(112 + i);
        }
        red_out<true>(rsx, rso, L, hf, x2o + hf * 32 * KB, s2o + hf * 4 * KB, g, c, hf, w);
        if (hf == 1) {
#pragma unroll
          for (int i = WKV_LOOP; i < 16; ++i) wkv[i] = LDK(rsn, i);
        }
      }
    }
    STAMP(6 + 6 * l);
  }
  STAMP_FLUSH;
}
__global__ __launch_bounds__(512, 2) void k_main(unsigned char* ws, const uint16_t* xin, uint16_t* out) {
  extern __shared__ __attribute__((aligned(16))) unsigned char smem[];
  LAS unsigned char* L = (LAS unsigned char*)smem;
  if (__builtin_amdgcn_readfirstlane(threadIdx.x >> 6) < 4) run<0>(ws, xin, out, L);
  else run<1>(ws, xin, out, L);
}
}

extern "C" void kernel_launch(void* const* d_in, const int* in_sizes, int n_in, void* d_out, int out_size, void* d_ws, size_t ws_size, hipStream_t stream) {
  const uint16_t* x = (const uint16_t*)d_in[0];
  const uint16_t* wqkv = (const uint16_t*)d_in[1];
  const uint16_t* wo = (const uint16_t*)d_in[2];
  const uint16_t* w1 = (const uint16_t*)d_in[4];
  const uint16_t* w2 = (const uint16_t*)d_in[5];
  unsigned char* ws = (unsigned char*)d_ws;
  uint16_t* out = (uint16_t*)d_out;
  (void)hipFuncSetAttribute((const void*)k_main, hipFuncAttributeMaxDynamicSharedMemorySize, LDS_BYTES);
  k_prep<<<PREP_BLOCKS, 256, 0, stream>>>(wqkv, wo, w1, w2, ws);
  k_main<<<256, 512, LDS_BYTES, stream>>>(ws, x, out);
}
```

```cpp
#include <hip/hip_runtime.h>
#include <hip/hip_bf16.h>
#include <stdint.h>

#define GAS __attribute__((address_space(1)))
#define LAS __attribute__((address_space(3)))
typedef unsigned v4u __attribute__((ext_vector_type(4)));
typedef unsigned v2u __attribute__((ext_vector_type(2)));
typedef float f32x4 __attribute__((ext_vector_type(4)));
typedef float f32x2 __attribute__((ext_vector_type(2)));
typedef __bf16 bf16x8 __attribute__((ext_vector_type(8)));
typedef __bf16 bf16x2 __attribute__((ext_vector_type(2)));
typedef short s16x4 __attribute__((ext_vector_type(4)));

#define STAMP(slot)
#define STAMP_DECL
#define STAMP_FLUSH

namespace {
constexpr int S = 2048, D = 512, F = 1792, H = 32;
constexpr int NG = 32;
constexpr int KB = 1024;
constexpr unsigned SENT = 0xFFFFFFFFu;
constexpr int OFF_WQKV = 0;
constexpr int OFF_WO = OFF_WQKV + 96 * 16 * KB;
constexpr int OFF_W1 = OFF_WO + 32 * 16 * KB;
constexpr int OFF_W2 = OFF_W1 + 112 * 16 * KB;
constexpr int OFF_CSQ = OFF_W2 + 32 * 56 * KB;
constexpr int OFF_CS1 = OFF_CSQ + 8 * KB;
constexpr int OFF_TMO = OFF_CS1 + 8 * KB;
constexpr int XBYTES = NG * 64 * KB;
constexpr int STBYTES = NG * 8 * KB;
constexpr int OFF_X0 = OFF_TMO + 4 * KB;
constexpr int OFF_ST0 = OFF_X0 + XBYTES;
constexpr int OFF_FILL0 = OFF_ST0 + STBYTES;
constexpr int KVPBYTES = NG * H * KB;
constexpr int OFF_KVP = OFF_FILL0;
constexpr int ZPBYTES = NG * H * 64;
constexpr int OFF_ZP = OFF_KVP + 2 * KVPBYTES;
constexpr int KVRBYTES = 16 * KB;
constexpr int OFF_KVR = OFF_ZP + 2 * ZPBYTES;
constexpr int ZRBYTES = KB;
constexpr int OFF_ZR = OFF_KVR + 2 * KVRBYTES;
constexpr int OFF_ST1 = OFF_ZR + 2 * ZRBYTES;
constexpr int OFF_PQ0 = OFF_ST1 + 3 * STBYTES;
constexpr int OFF_X1 = OFF_PQ0 + XBYTES;
#define FILL_MAIN 1
constexpr int OFF_PQ1 = OFF_X1 + 3 * XBYTES;
constexpr int HBYTES = NG * 224 * KB;
constexpr int OFF_H = OFF_PQ1 + XBYTES;
constexpr int WS_TOTAL = OFF_H + 2 * HBYTES;
constexpr int OFF_FILL1 = FILL_MAIN ? OFF_X1 + XBYTES : WS_TOTAL;
static_assert((OFF_FILL0 % 1024) == 0 && (OFF_FILL1 % 1024) == 0, "");
__host__ __device__ constexpr int xoff(int xi) { return xi == 0 ? OFF_X0 : OFF_X1 + (xi - 1) * XBYTES; }
__host__ __device__ constexpr int stoff(int xi) { return xi == 0 ? OFF_ST0 : OFF_ST1 + (xi - 1) * STBYTES; }

__device__ __forceinline__ f32x4 mfma32(v4u a, v4u b, f32x4 c) {
  return __builtin_amdgcn_mfma_f32_16x16x32_bf16(__builtin_bit_cast(bf16x8, a), __builtin_bit_cast(bf16x8, b), c, 0, 0, 0);
}
__device__ __forceinline__ f32x4 mfma16(v2u a, v2u b, f32x4 c) {
  return __builtin_amdgcn_mfma_f32_16x16x16bf16_1k(__builtin_bit_cast(s16x4, a), __builtin_bit_cast(s16x4, b), c, 0, 0, 0);
}
__device__ __forceinline__ unsigned pk2(float lo, float hi) {
  bf16x2 v; v.x = (__bf16)lo; v.y = (__bf16)hi;
  return __builtin_bit_cast(unsigned, v);
}
__device__ __forceinline__ unsigned san(unsigned u) { return u < 0xFFFFFFFEu ? u : 0xFFFFFFFEu; }
__device__ __forceinline__ unsigned pk2s(float lo, float hi) { return san(pk2(lo, hi)); }
__device__ __forceinline__ float asf(unsigned u) { return __builtin_bit_cast(float, u); }
__device__ __forceinline__ float bflo(unsigned u) { return __builtin_bit_cast(float, u << 16); }
__device__ __forceinline__ float bfhi(unsigned u) { return __builtin_bit_cast(float, u & 0xFFFF0000u); }
__device__ __forceinline__ float fexp(float t) { return __builtin_amdgcn_exp2f(t * 1.44269504088896f); }
__device__ __forceinline__ float phi(float t) { return fmaxf(t, 0.f) + fexp(fminf(t, 0.f)) + 1e-6f; }
__device__ __forceinline__ int permk(int h, int i) { return i < 4 ? 4 * h + i : 16 + 4 * h + (i - 4); }
#define XMAP 2
__device__ __forceinline__ void blk_gc(int b, int& g, int& c) {
  const int xcd = b & 7, idx = b >> 3;
  if (XMAP == 1) { g = xcd * 4 + (idx >> 3); c = idx & 7; }
  else if (XMAP == 2) { g = (xcd >> 1) * 8 + (idx >> 2); c = (xcd & 1) * 4 + (idx & 3); }
  else { g = (xcd >> 2) * 16 + (idx >> 1); c = (xcd & 3) * 2 + (idx & 1); }
}
__device__ __forceinline__ unsigned umax(unsigned a, unsigned b) { return a > b ? a : b; }
__device__ __forceinline__ unsigned umax4(v4u v) { return umax(umax(v.x, v.y), umax(v.z, v.w)); }
#define SB() __builtin_amdgcn_sched_barrier(0)

constexpr int PREP_BLOCKS = 352;
__global__ __launch_bounds__(256) void k_prep(const uint16_t* __restrict__ wqkv, const uint16_t* __restrict__ wo,
                                              const uint16_t* __restrict__ w1, const uint16_t* __restrict__ w2, unsigned char* __restrict__ ws) {
  __shared__ __attribute__((aligned(16))) unsigned char sm[4 * 32 * 144];
  const int tid = threadIdx.x, lane = tid & 63, w = __builtin_amdgcn_readfirstlane(tid >> 6);
  int id = blockIdx.x * 4 + w;
  const uint16_t* W; int N, off, UT, u, ng;
  if (id < 384) { W = wqkv; N = 1536; off = OFF_WQKV; UT = 16; u = id & 15; ng = id >> 4; }
  else if (id < 512) { id -= 384; W = wo; N = 512; off = OFF_WO; UT = 16; u = id & 15; ng = id >> 4; }
  else if (id < 960) { id -= 512; W = w1; N = 1792; off = OFF_W1; UT = 16; u = id & 15; ng = id >> 4; }
  else { id -= 960; W = w2; N = 512; off = OFF_W2; UT = 56; u = id % 56; ng = id / 56; }
  v4u v[4];
#pragma unroll
  for (int q = 0; q < 4; ++q) v[q] = *(const v4u*)(W + (size_t)(32 * u + 8 * q + (lane >> 3)) * N + ng * 64 + (lane & 7) * 8);
  {
    const int n16 = (OFF_FILL1 - OFF_FILL0) / 16;
    v4u* p = (v4u*)(ws + OFF_FILL0);
    for (int i = blockIdx.x * 256 + tid; i < n16; i += PREP_BLOCKS * 256) p[i] = v4u{SENT, SENT, SENT, SENT};
    if (blockIdx.x == 0 && tid == 0) *(unsigned*)(ws + OFF_TMO) = 0u;
  }
  LAS unsigned char* S = (LAS unsigned char*)sm + w * (32 * 144);
#pragma unroll
  for (int q = 0; q < 4; ++q) *(LAS v4u*)(S + (8 * q + (lane >> 3)) * 144 + (lane & 7) * 16) = v[q];
  const int h = lane >> 4, qq = (lane & 15) >> 2, p = lane & 3;
#pragma unroll
  for (int t = 0; t < 4; ++t) {
    const s16x4 lo = __builtin_amdgcn_ds_read_tr16_b64_v4i16((LAS s16x4*)(S + (4 * h + qq) * 144 + (16 * t + 4 * p) * 2));
    const s16x4 hi = __builtin_amdgcn_ds_read_tr16_b64_v4i16((LAS s16x4*)(S + (16 + 4 * h + qq) * 144 + (16 * t + 4 * p) * 2));
    const v2u a = __builtin_bit_cast(v2u, lo), b = __builtin_bit_cast(v2u, hi);
    *(v4u*)(ws + off + ((size_t)((ng * 4 + t) * UT + u) * 64 + lane) * 16) = v4u{a.x, a.y, b.x, b.y};
  }
}

typedef __amdgpu_buffer_rsrc_t rsrc_t;
constexpr int SC1 = 16;
__device__ __forceinline__ v4u ld128(rsrc_t rs, int voff, int soff) { return __builtin_amdgcn_raw_buffer_load_b128(rs, voff, soff, 0); }
__device__ __forceinline__ v4u ld128c(rsrc_t rs, int voff, int soff) { return __builtin_amdgcn_raw_buffer_load_b128(rs, voff, soff, SC1); }
__device__ __forceinline__ v2u ld64c(rsrc_t rs, int voff, int soff) { return __builtin_amdgcn_raw_buffer_load_b64(rs, voff, soff, SC1); }
__device__ __forceinline__ unsigned ld32c(rsrc_t rs, int voff, int soff) { return __builtin_amdgcn_raw_buffer_load_b32(rs, voff, soff, SC1); }
__device__ __forceinline__ void st128c(rsrc_t rs, v4u v, int voff, int soff) { __builtin_amdgcn_raw_buffer_store_b128(v, rs, (int)((unsigned)voff + (unsigned)soff), 0, SC1); }
__device__ __forceinline__ void st64c(rsrc_t rs, v2u v, int voff, int soff) { __builtin_amdgcn_raw_buffer_store_b64(v, rs, voff, soff, SC1); }
__device__ __forceinline__ void st32c(rsrc_t rs, unsigned v, int voff, int soff) { __builtin_amdgcn_raw_buffer_store_b32(v, rs, voff, soff, SC1); }
__device__ __forceinline__ bool valid(unsigned m) { return __builtin_amdgcn_ballot_w64(m == SENT) == 0ull; }
__device__ __forceinline__ unsigned asu(float f) { return __builtin_bit_cast(unsigned, f); }
template <int N> __device__ __forceinline__ float ror_add(float x) {
  return x + asf((unsigned)__builtin_amdgcn_update_dpp(0, (int)asu(x), 0x120 | N, 0xF, 0xF, false));
}
__device__ __forceinline__ float sum_x16(float x) { auto r = __builtin_amdgcn_permlane16_swap(asu(x), asu(x), false, false); return asf(r[0]) + asf(r[1]); }
__device__ __forceinline__ float sum_x32(float x) { auto r = __builtin_amdgcn_permlane32_swap(asu(x), asu(x), false, false); return asf(r[0]) + asf(r[1]); }
constexpr unsigned SPIN_CAP = 1u << 14;
__device__ __forceinline__ bool spin_fail(unsigned& n, rsrc_t rs) {
  asm volatile("" ::: "memory");
  __builtin_amdgcn_s_sleep(2);
  ++n;
  if (n > SPIN_CAP) { st32c(rs, 1u, OFF_TMO, 0); return true; }
  if ((n & 63u) == 0u) { if (__builtin_amdgcn_readfirstlane(ld32c(rs, OFF_TMO, 0)) != 0) return true; }
  return false;
}

#define DUTY_POS 0
#define W1_G 2
#define POLL_K0 2
#define PQ_K 3
#define WKV_LOOP 10
#define PQ_SLEEP 32
#define ROLE_ORD 1
#define W2X 6
#define WO_G1 1
#define W1_G1 0
#define FILL_HF 0
#define PQ_POS 1
#define X1_POS 2
#define WQQ_POS 2
#define H1_POS 1
#define H1_U 3
#define W1_Q 6
#define XI_U 16
#define W1_EARLY 14
#define KVR_SLEEP 4
#define W1_MID 6
#define W2_EARLY 8
#define W2_WO 0
constexpr int LDS_REG = 64 * KB;
constexpr int LDS_STAT = 2 * LDS_REG;
constexpr int LDS_CS = LDS_STAT + 512;
constexpr int LDS_RES = LDS_CS + 2048;
constexpr int LDS_VX = LDS_RES + 8 * KB;
constexpr int LDS_KV = LDS_VX;
constexpr int LDS_BYTES = LDS_VX + 17 * KB;
constexpr int OOBV = 0x7FFF0000;

__device__ __forceinline__ int fresh_lane() {
  unsigned z;
  asm volatile("s_mov_b32 %0, 0" : "=s"(z));
  return (int)__builtin_amdgcn_mbcnt_hi(~0u, __builtin_amdgcn_mbcnt_lo(~0u, z));
}
#define OPQ(x) asm volatile("" : "+s"(x))

__device__ __forceinline__ unsigned hmax(const v4u (&a)[14]) {
  unsigned m = 0;
#pragma unroll
  for (int i = 0; i < 14; ++i) m = umax(m, umax4(a[i]));
  return m;
}
__device__ __forceinline__ void reld128(v4u& a, const unsigned char* base, unsigned off) {
  asm volatile("s_nop 4\n\tglobal_load_dwordx4 %0, %1, %2 sc1" : "+v"(a) : "v"(off), "s"(base) : "memory");
}
__device__ __forceinline__ void reld64(v2u& a, const unsigned char* base, unsigned off) {
  asm volatile("s_nop 4\n\tglobal_load_dwordx2 %0, %1, %2 sc1" : "+v"(a) : "v"(off), "s"(base) : "memory");
}
struct XS { v4u x[4]; v2u s; };
__device__ __forceinline__ void x_issue(XS& a, rsrc_t rs, int xo, int so) {
  const int ln = fresh_lane();
#pragma unroll
  for (int i = 0; i < 4; ++i) a.x[i] = ld128c(rs, ln * 16 + i * KB, xo);
  a.s = ld64c(rs, ln * 8, so);
}
__device__ __forceinline__ unsigned x_max(const XS& a) { return umax(umax(umax4(a.x[0]), umax4(a.x[1])), umax(umax(umax4(a.x[2]), umax4(a.x[3])), umax(a.s.x, a.s.y))); }
__device__ __forceinline__ void x_finish(XS& a, rsrc_t rs, const unsigned char* ws, int xo, int so, LAS unsigned char* L, int par, int w) {
  if (__builtin_expect(!valid(x_max(a)), 0)) {
    unsigned n = 0;
    do {
      if (spin_fail(n, rs)) break;
      const unsigned ln = (unsigned)fresh_lane();
#pragma unroll
      for (int i = 0; i < 4; ++i) reld128(a.x[i], ws, ln * 16 + xo + i * KB);
      reld64(a.s, ws, ln * 8 + so);
      asm volatile("s_waitcnt vmcnt(0)" : "+v"(a.x[0]), "+v"(a.x[1]), "+v"(a.x[2]), "+v"(a.x[3]), "+v"(a.s) :: "memory");
    } while (!valid(x_max(a)));
  }
  const int ln = fresh_lane();
  LAS unsigned char* dst = L + par * LDS_REG + w * 4096 + ln * 16;
#pragma unroll
  for (int i = 0; i < 4; ++i) *(LAS v4u*)(dst + i * KB) = a.x[i];
  const float mp = asf(a.s.x), m2p = asf(a.s.y);
  const float mean = ror_add<1>(ror_add<2>(ror_add<4>(ror_add<8>(mp)))) * (1.f / 16.f);
  const float dm = mp - mean;
  const float m2 = ror_add<1>(ror_add<2>(ror_add<4>(ror_add<8>(m2p + 32.f * dm * dm))));
  const float rstd = __builtin_amdgcn_rsqf(m2 * (1.f / 512.f) + 1e-5f);
  if ((ln & 15) == 0) {
    LAS float* st = (LAS float*)(L + LDS_STAT + par * 256) + w * 4 + (ln >> 4);
    st[0] = mean; st[32] = rstd;
  }
  __syncthreads();
}

__device__ __forceinline__ void xraw_issue(XS& a, rsrc_t rx, int row0) {
  const int ln = fresh_lane();
#pragma unroll
  for (int i = 0; i < 4; ++i) a.x[i] = ld128(rx, ln * 16 + i * KB, row0 * KB);
}
__device__ __forceinline__ float wave_sum(float x) { return sum_x32(sum_x16(ror_add<1>(ror_add<2>(ror_add<4>(ror_add<8>(x)))))); }
__device__ __forceinline__ void xraw_finish(XS& a, LAS unsigned char* L, int par, int w) {
  const int ln = fresh_lane();
  const int u = ln >> 2, q = ln & 3;
#pragma unroll
  for (int i = 0; i < 4; ++i) {
    const int tok = 4 * w + i;
    LAS unsigned char* d = L + par * LDS_REG + (u * 2 + (tok >> 4)) * KB + ((tok & 15) + 32 * (q & 1)) * 16 + (q >> 1) * 8;
    *(LAS v2u*)d = v2u{a.x[i].x, a.x[i].y};
    *(LAS v2u*)(d + 256) = v2u{a.x[i].z, a.x[i].w};
    const float v[8] = {bflo(a.x[i].x), bfhi(a.x[i].x), bflo(a.x[i].y), bfhi(a.x[i].y), bflo(a.x[i].z), bfhi(a.x[i].z), bflo(a.x[i].w), bfhi(a.x[i].w)};
    const float mean = wave_sum(((v[0] + v[1]) + (v[2] + v[3])) + ((v[4] + v[5]) + (v[6] + v[7]))) * (1.f / 512.f);
    float m2 = 0.f;
#pragma unroll
    for (int e = 0; e < 8; ++e) { const float dd = v[e] - mean; m2 += dd * dd; }
    m2 = wave_sum(m2);
    if (ln == 0) {
      LAS float* st = (LAS float*)(L + LDS_STAT + par * 256) + tok;
      st[0] = mean; st[32] = __builtin_amdgcn_rsqf(m2 * (1.f / 512.f) + 1e-5f);
    }
  }
  __syncthreads();
}

__device__ __forceinline__ void red_write(f32x4 (&acc)[4][2], LAS unsigned char* L, int par, int w) {
  const int ln = fresh_lane();
  LAS f32x4* red = (LAS f32x4*)(L + par * LDS_REG + w * 8 * KB + ln * 16);
#pragma unroll
  for (int nt = 0; nt < 4; ++nt)
#pragma unroll
    for (int mm = 0; mm < 2; ++mm) red[(nt * 2 + mm) * 64] = acc[nt][mm];
}
template <bool LAST>
__device__ __forceinline__ void red_out(rsrc_t rsw, rsrc_t rso, LAS unsigned char* L, int par, int xo, int so, int g, int c, int hf, int w) {
  const int ln = fresh_lane();
  const int mm = (w >> 1) & 1, uu = w & 1;
  const int j = ln & 15, h = ln >> 4;
  v4u o = {0, 0, 0, 0};
  v2u stv = {0, 0};
  if (w < 4) {
    const LAS f32x4* red = (const LAS f32x4*)(L + par * LDS_REG + (4 * uu + mm) * KB + ln * 16);
    LAS v4u* resp = (LAS v4u*)(L + LDS_RES + (hf * 4 + w) * KB + ln * 16);
    const v4u resid = *resp;
    f32x4 s0 = {0, 0, 0, 0}, s1 = {0, 0, 0, 0};
#pragma unroll
    for (int ww = 0; ww < 8; ++ww) {
      s0 += red[ww * 512];
      s1 += red[ww * 512 + 128];
    }
    s0.x += bflo(resid.x); s0.y += bfhi(resid.x); s0.z += bflo(resid.y); s0.w += bfhi(resid.y);
    s1.x += bflo(resid.z); s1.y += bfhi(resid.z); s1.z += bflo(resid.w); s1.w += bfhi(resid.w);
    o = v4u{pk2s(s0.x, s0.y), pk2s(s0.z, s0.w), pk2s(s1.x, s1.y), pk2s(s1.z, s1.w)};
    *resp = o;
  }
  asm volatile("" : "+v"(o));
  st128c(rsw, o, ln * 16, xo + ((2 * c + uu) * 2 + mm) * KB);
  if constexpr (LAST) {
    const int voff = j * (D * 2) + h * 8, soff = ((g * 64 + hf * 32 + 16 * mm) * D + 64 * c + 32 * uu) * 2;
    __builtin_amdgcn_raw_buffer_store_b64(v2u{o.x, o.y}, rso, voff, soff, 0);
    __builtin_amdgcn_raw_buffer_store_b64(v2u{o.z, o.w}, rso, voff, soff + 32, 0);
  }
  if (w < 4) {
    float v[8] = {bflo(o.x), bfhi(o.x), bflo(o.y), bfhi(o.y), bflo(o.z), bfhi(o.z), bflo(o.w), bfhi(o.w)};
    const float s = sum_x32(sum_x16(((v[0] + v[1]) + (v[2] + v[3])) + ((v[4] + v[5]) + (v[6] + v[7]))));
    const float mean = s * (1.f / 32.f);
    float m2 = 0.f;
#pragma unroll
    for (int i = 0; i < 8; ++i) { float d = v[i] - mean; m2 += d * d; }
    m2 = sum_x32(sum_x16(m2));
    stv = v2u{san(asu(mean)), san(asu(m2))};
  }
  asm volatile("" : "+v"(stv));
  st64c(rsw, stv, h == 0 ? j * 128 : OOBV, so + (16 * mm * 16 + 2 * c + uu) * 8);
}

template <int G>
__device__ __forceinline__ void run(unsigned char* ws, const uint16_t* xin, uint16_t* out, LAS unsigned char* L) {
  const int w = G * 4 + (__builtin_amdgcn_readfirstlane(threadIdx.x >> 6) & 3);
  int g, c; blk_gc(blockIdx.x, g, c);
  const rsrc_t rs = __builtin_amdgcn_make_buffer_rsrc((void*)ws, 0, WS_TOTAL, 0x00020000);
  const rsrc_t rx = __builtin_amdgcn_make_buffer_rsrc((void*)xin, 0, S * D * 2, 0x00020000);
  const rsrc_t rsnull = __builtin_amdgcn_make_buffer_rsrc((void*)ws, 0, 0, 0x00020000);
  const f32x4 zero = {0, 0, 0, 0};
  const int hd = 4 * c + (w & 3);
  int skv = OFF_WQKV + ((w < 4 ? 32 : 64) + hd) * 16 * KB;
  int sq_ = OFF_WQKV + hd * 16 * KB;
  int so_ = OFF_WO + (4 * c * 16 + 2 * w) * KB;
  int s1_ = OFF_W1 + (14 * c + 2 * (w < 7 ? w : 6)) * 16 * KB;
  int s2_ = OFF_W2 + (4 * c * 56 + 7 * w) * KB;
  int sx_ = w * 4096 + g * 64 * KB;
  int ss_ = w * 512 + g * 8 * KB;
  const rsrc_t rs1 = __builtin_amdgcn_make_buffer_rsrc((void*)ws, 0, w < 7 ? WS_TOTAL : 0, 0x00020000);
  const rsrc_t rsk = __builtin_amdgcn_make_buffer_rsrc((void*)ws, 0, w < 4 ? WS_TOTAL : 0, 0x00020000);
  STAMP_DECL
  STAMP(0);
#define LDO(i) ld128(rs, l16 + ((i) & 1) * KB, so_ + ((i) >> 1) * 16 * KB)
#define LD1(i) ld128(rs, l16 + ((i) & 3) * KB, s1_ + ((i) >> 2) * 4 * KB)
#define LD1N(i) ld128(rs1, l16 + ((i) & 3) * KB, s1_ + ((i) >> 2) * 4 * KB)
#define LD2(i) ld128(rs, l16 + (((i) >> 2) & 3) * KB, s2_ + (((i) & 3) * 56 + (((i) >> 2) & 4)) * KB)
#define LDK(r_, i) ld128(r_, l16 + ((i) & 3) * KB, skv + ((i) >> 2) * 4 * KB)
#define LDH(i) ld128c(rs, l16 + ((i) & 3) * KB, sh_ + ((i) >> 2) * 4 * KB)
  v4u wkv[16];
  XS xs;
  {
    const int lane = fresh_lane(), l16 = lane * 16;
    xraw_issue(xs, rx, g * 64 + 4 * w);
#pragma unroll
    for (int i = 0; i < 16; ++i) wkv[i] = LDK(rs, i);
    if (w < 4) {
#pragma unroll
      for (int hf = 0; hf < 2; ++hf) {
        const int vo_ = (lane & 15) * KB + (lane >> 4) * 8, so2 = (g * 64 + hf * 32 + 16 * (w >> 1)) * KB + (64 * c + 32 * (w & 1)) * 2;
        const v2u a = __builtin_amdgcn_raw_buffer_load_b64(rx, vo_, so2, 0), b = __builtin_amdgcn_raw_buffer_load_b64(rx, vo_ + 32, so2, 0);
        *(LAS v4u*)(L + LDS_RES + (hf * 4 + w) * KB + l16) = v4u{a.x, a.y, b.x, b.y};
      }
    }
  }
  LAS float* csl = (LAS float*)(L + LDS_CS);
  const v4u ones4 = {0x3F803F80u, 0x3F803F80u, 0x3F803F80u, 0x3F803F80u};
  auto fill_group = [&](rsrc_t rf, int layer) {
    const int l16 = fresh_lane() * 16;
    const v4u sv = {SENT, SENT, SENT, SENT};
#pragma unroll
    for (int i = 0; i < 7; ++i) {
      const int f = 7 * (w & 3) + i;
      st128c(rf, sv, l16, OFF_H + layer * HBYTES + (((g * 2 + f / 14) * 56 + 7 * c + (f % 14) / 2) * 2 + (f & 1)) * KB);
    }
#pragma unroll
    for (int hf = 0; hf < 2; ++hf) st128c(rf, sv, l16, OFF_X1 + (1 + layer) * XBYTES + (g * 2 + hf) * 32 * KB + ((2 * c + (w & 1)) * 2 + ((w >> 1) & 1)) * KB);
    if (layer == 0) {
#pragma unroll
      for (int hf = 0; hf < 2; ++hf) st128c(rf, sv, l16, OFF_PQ1 + ((g * 2 + hf) * H + hd) * KB);
    }
  };
  SB();
#pragma unroll 1
  for (int l = 0; l < 2; ++l) {
    v4u wqq[16], wo[8], w1[32], w2[28];
    OPQ(skv); OPQ(sq_); OPQ(so_); OPQ(s1_); OPQ(s2_); OPQ(sx_); OPQ(ss_);
    {
      const int xo = OFF_X1 + XBYTES + sx_, so = OFF_ST1 + STBYTES + ss_;
      v4u fr[2];
#pragma unroll
      for (int hf = 0; hf < 2; ++hf) {
        if (l == 0) {
          if (hf == 0) {
            f32x4 cs = zero;
#pragma unroll
            for (int u = 0; u < 16; ++u) cs = mfma32(ones4, wkv[u], cs);
            const int ln = fresh_lane();
            if (ln < 16) csl[64 + 16 * w + ln] = cs.x;
          }
          xraw_finish(xs, L, hf, w);
        } else x_finish(xs, rs, ws, xo + hf * 32 * KB, so + hf * 4 * KB, L, hf, w);
        const int lane = fresh_lane(), l16 = lane * 16, j = lane & 15, h = lane >> 4;
        const LAS v4u* xl = (const LAS v4u*)(L + hf * LDS_REG + l16);
        const LAS float* st = (const LAS float*)(L + LDS_STAT + hf * 256);
        v4u xr[4][2];
#pragma unroll
        for (int u = 0; u < 4; ++u) { xr[u][0] = xl[(u * 2) * 64]; xr[u][1] = xl[(u * 2 + 1) * 64]; }
        if (ROLE_ORD && G == 1) {
#pragma unroll
          for (int i = 0; i < 8; ++i) wqq[hf * 8 + i] = ld128(rs, l16 + (i & 3) * KB, sq_ + (hf * 8 + (i & 4)) * KB);
          if (WO_G1 == hf) {
#pragma unroll
            for (int i = 0; i < 8; ++i) wo[i] = LDO(i);
          }
        }
        SB();
        f32x4 a[2] = {zero, zero};
#pragma unroll
        for (int u = 0; u < 16; ++u) {
          a[0] = mfma32(xr[u % 4][0], wkv[u], a[0]);
          a[1] = mfma32(xr[u % 4][1], wkv[u], a[1]);
          if (u + 4 < 16) { xr[u % 4][0] = xl[((u + 4) * 2) * 64]; xr[u % 4][1] = xl[((u + 4) * 2 + 1) * 64]; }
          if (hf == 0 && u == XI_U) { if (l == 0) xraw_issue(xs, rx, g * 64 + 32 + 4 * w); else x_issue(xs, rs, xo + 32 * KB, so + 4 * KB); }
          if (WQQ_POS == 1 && (u & 1) == 1) wqq[hf * 8 + (u >> 1)] = ld128(rs, l16 + ((u >> 1) & 3) * KB, sq_ + (hf * 8 + ((u >> 1) & 4)) * KB);
          SB();
        }
        if (hf == 0 && XI_U == 16) { if (l == 0) xraw_issue(xs, rx, g * 64 + 32 + 4 * w); else x_issue(xs, rs, xo + 32 * KB, so + 4 * KB); }
        if (WQQ_POS == 2 && !(ROLE_ORD && G == 1)) {
#pragma unroll
          for (int i = 0; i < 8; ++i) wqq[hf * 8 + i] = ld128(rs, l16 + (i & 3) * KB, sq_ + (hf * 8 + (i & 4)) * KB);
        }
        if (hf == 1 && !(ROLE_ORD && G == 1)) {
#pragma unroll
          for (int i = 0; i < 8; ++i) wo[i] = LDO(i);
        }
        const float cs = csl[64 + 16 * w + j];
        unsigned f[4];
#pragma unroll
        for (int mm = 0; mm < 2; ++mm) {
          const f32x4 mean = *(const LAS f32x4*)(st + 16 * mm + 4 * h), rstd = *(const LAS f32x4*)(st + 32 + 16 * mm + 4 * h);
          float t[4];
#pragma unroll
          for (int r = 0; r < 4; ++r) { const float tt = (a[mm][r] - mean[r] * cs) * rstd[r]; t[r] = w < 4 ? phi(tt) : tt; }
          f[2 * mm] = pk2(t[0], t[1]); f[2 * mm + 1] = pk2(t[2], t[3]);
        }
        fr[hf] = v4u{f[0], f[1], f[2], f[3]};
        if (FILL_MAIN && G == 0 && hf == FILL_HF) fill_group(__builtin_amdgcn_make_buffer_rsrc((void*)ws, 0, l == 0 ? WS_TOTAL : 0, 0x00020000), 0);
      }
      const int lane = fresh_lane(), l16 = lane * 16, j = lane & 15, h = lane >> 4;
      LAS v4u* vx = (LAS v4u*)(L + LDS_VX + (w & 3) * 2 * KB + l16);
      if (w >= 4) { vx[0] = fr[0]; vx[64] = fr[1]; }
      __syncthreads();
      {
        f32x4 kv = zero, z = zero;
        if (w < 4) {
          kv = mfma32(fr[0], vx[0], kv); z = mfma32(fr[0], ones4, z);
          kv = mfma32(fr[1], vx[64], kv); z = mfma32(fr[1], ones4, z);
        }
        asm volatile("" : "+v"(kv), "+v"(z));
        const v4u kvu = __builtin_bit_cast(v4u, kv), zu = __builtin_bit_cast(v4u, z);
        st128c(rsk, v4u{san(kvu.x), san(kvu.y), san(kvu.z), san(kvu.w)}, l16, OFF_KVP + l * KVPBYTES + (g * H + hd) * KB);
        st128c(rsk, v4u{san(zu.x), san(zu.y), san(zu.z), san(zu.w)}, j == 0 ? h * 16 : OOBV, OFF_ZP + l * ZPBYTES + (g * H + hd) * 64);
      }
      SB();
      {
        const int hf = w >> 2;
        const LAS v4u* xl = (const LAS v4u*)(L + hf * LDS_REG + l16);
        const LAS float* st = (const LAS float*)(L + LDS_STAT + hf * 256);
        v4u xr[4][2];
#pragma unroll
        for (int u = 0; u < 4; ++u) { xr[u][0] = xl[(u * 2) * 64]; xr[u][1] = xl[(u * 2 + 1) * 64]; }
        SB();
        if (ROLE_ORD && G == 1 && WO_G1 == 2) {
#pragma unroll
          for (int i = 0; i < 8; ++i) wo[i] = LDO(i);
        }
        w1[0] = LD1N(0); w1[16] = LD1N(16); w1[1] = LD1N(1); w1[17] = LD1N(17);
        if (l == 0) {
          f32x4 cs = zero;
#pragma unroll
          for (int u = 0; u < 16; ++u) cs = mfma32(wqq[u], ones4, cs);
          if (j == 0) *(LAS f32x4*)(csl + 16 * (w & 3) + 4 * h) = cs;
        }
        f32x4 aq[2] = {zero, zero};
#pragma unroll
        for (int u = 0; u < 16; ++u) {
          aq[0] = mfma32(wqq[u], xr[u % 4][0], aq[0]);
          aq[1] = mfma32(wqq[u], xr[u % 4][1], aq[1]);
          if (u + 4 < 16) { xr[u % 4][0] = xl[((u + 4) * 2) * 64]; xr[u % 4][1] = xl[((u + 4) * 2 + 1) * 64]; }
          if (u < 12) { const int t = u >> 1; if ((u & 1) == 0) w1[2 + t] = LD1N(2 + t); else w1[18 + t] = LD1N(18 + t); }
          SB();
        }
        unsigned pq[4];
        const f32x4 csq = *(const LAS f32x4*)(csl + 16 * (w & 3) + 4 * h);
#pragma unroll
        for (int mm = 0; mm < 2; ++mm) {
          const float mean = st[16 * mm + j], rstd = st[32 + 16 * mm + j];
          float q[4];
#pragma unroll
          for (int r = 0; r < 4; ++r) q[r] = phi((aq[mm][r] - mean * csq[r]) * rstd);
          pq[2 * mm] = pk2s(q[0], q[1]); pq[2 * mm + 1] = pk2s(q[2], q[3]);
        }
        st128c(rs, v4u{pq[0], pq[1], pq[2], pq[3]}, l16, (l == 0 ? OFF_PQ0 : OFF_PQ1) + ((g * 2 + hf) * H + hd) * KB);
        SB();
      }
    }
    STAMP(1 + 6 * l);
    if (w == 4) {
      const int lane = fresh_lane();
      const int ch = lane & 7, gq = lane >> 3;
      v4u p[4]; unsigned n = 0;
      for (;;) {
#pragma unroll
        for (int i = 0; i < 4; ++i) p[i] = ld128c(rs, gq * H * KB + ch * 16, OFF_KVP + l * KVPBYTES + (i * 8 * H + g) * KB + c * 128);
        if (valid(umax(umax(umax4(p[0]), umax4(p[1])), umax(umax4(p[2]), umax4(p[3]))))) break;
        if (spin_fail(n, rs)) break;
      }
      f32x4 s = (__builtin_bit_cast(f32x4, p[0]) + __builtin_bit_cast(f32x4, p[1])) + (__builtin_bit_cast(f32x4, p[2]) + __builtin_bit_cast(f32x4, p[3]));
#pragma unroll
      for (int r = 0; r < 4; ++r) s[r] = sum_x32(sum_x16(ror_add<8>(s[r])));
      if (lane < 8) st64c(rs, v2u{pk2s(s.x, s.y), pk2s(s.z, s.w)}, lane * 16, OFF_KVR + l * KVRBYTES + ((g >> 1) * 64 + 8 * c) * 16 + (g & 1) * 8);
    } else if (w == 5 && c == 0) {
      const int lane = fresh_lane();
      const int hh = lane & 3, gq = lane >> 2;
      v4u p[2]; unsigned n = 0;
      for (;;) {
#pragma unroll
        for (int i = 0; i < 2; ++i) p[i] = ld128c(rs, gq * H * 64 + hh * 16, OFF_ZP + l * ZPBYTES + (i * 16 * H + g) * 64);
        if (valid(umax(umax4(p[0]), umax4(p[1])))) break;
        if (spin_fail(n, rs)) break;
      }
      f32x4 s = __builtin_bit_cast(f32x4, p[0]) + __builtin_bit_cast(f32x4, p[1]);
#pragma unroll
      for (int r = 0; r < 4; ++r) s[r] = sum_x32(sum_x16(ror_add<8>(ror_add<4>(s[r]))));
      if (lane < 4) st64c(rs, v2u{pk2s(s.x, s.y), pk2s(s.z, s.w)}, lane * 32, OFF_ZR + l * ZRBYTES + (g >> 2) * 128 + (g & 3) * 8);
    }
    {
      const int lane = fresh_lane(), l16 = lane * 16, h = lane >> 4;
      int sp_ = (l == 0 ? OFF_PQ0 : OFF_PQ1) + (g * 2 * H + 4 * w) * KB; OPQ(sp_);
      v4u pqa[2][4];
      __builtin_amdgcn_s_sleep(PQ_SLEEP);
#pragma unroll
      for (int hh = 0; hh < 4; ++hh) { pqa[0][hh] = ld128c(rs, l16 + hh * KB, sp_); pqa[1][hh] = ld128c(rs, l16 + hh * KB, sp_ + H * KB); }
      SB();
      if (w == 7) {
        v4u t[17];
        unsigned n = 0;
        for (;;) {
#pragma unroll
          for (int i = 0; i < 16; ++i) t[i] = ld128c(rs, l16 + (i & 3) * KB, OFF_KVR + l * KVRBYTES + (i >> 2) * 4 * KB);
          t[16] = ld128c(rs, l16, OFF_ZR + l * ZRBYTES);
          unsigned m = 0;
#pragma unroll
          for (int i = 0; i < 17; ++i) m = umax(m, umax4(t[i]));
          if (valid(m)) break;
          if (spin_fail(n, rs)) break;
        }
#pragma unroll
        for (int i = 0; i < 17; ++i) *(LAS v4u*)(L + LDS_KV + i * KB + l16) = t[i];
      }
      STAMP(2 + 6 * l);
      __syncthreads();
      v4u kvf[2], zf[2];
      kvf[0] = *(const LAS v4u*)(L + LDS_KV + 2 * w * KB + l16);
      kvf[1] = *(const LAS v4u*)(L + LDS_KV + (2 * w + 1) * KB + l16);
      zf[0] = *(const LAS v4u*)(L + LDS_KV + 16 * KB + w * 128 + h * 32);
      zf[1] = *(const LAS v4u*)(L + LDS_KV + 16 * KB + w * 128 + h * 32 + 16);
      const int x1o = OFF_X1 + 2 * l * XBYTES + g * 64 * KB, s1o = OFF_ST1 + 2 * l * STBYTES + g * 8 * KB;
#pragma unroll
      for (int hf = 0; hf < 2; ++hf) {
        v4u (&pq)[4] = pqa[hf];
        if (__builtin_expect(!valid(umax(umax(umax4(pq[0]), umax4(pq[1])), umax(umax4(pq[2]), umax4(pq[3])))), 0)) {
          unsigned n = 0;
          do {
            if (spin_fail(n, rs)) break;
#pragma unroll
            for (int hh = 0; hh < 4; ++hh) reld128(pq[hh], ws, l16 + hh * KB + sp_ + hf * H * KB);
            asm volatile("s_waitcnt vmcnt(0)" : "+v"(pq[0]), "+v"(pq[1]), "+v"(pq[2]), "+v"(pq[3]) :: "memory");
          } while (!valid(umax(umax(umax4(pq[0]), umax4(pq[1])), umax(umax4(pq[2]), umax4(pq[3])))));
        }
        v4u of[2][2];
#pragma unroll
        for (int hp = 0; hp < 2; ++hp) {
          f32x4 num[2][2], den[2][2];
#pragma unroll
          for (int t = 0; t < 2; ++t) {
            const v4u p = pq[hp * 2 + t];
            const v2u ka = t ? v2u{kvf[hp].z, kvf[hp].w} : v2u{kvf[hp].x, kvf[hp].y};
            const v2u za = t ? v2u{zf[hp].z, zf[hp].w} : v2u{zf[hp].x, zf[hp].y};
            num[t][0] = mfma16(ka, v2u{p.x, p.y}, zero); den[t][0] = mfma16(za, v2u{p.x, p.y}, zero);
            num[t][1] = mfma16(ka, v2u{p.z, p.w}, zero); den[t][1] = mfma16(za, v2u{p.z, p.w}, zero);
          }
          SB();
#pragma unroll
          for (int mm = 0; mm < 2; ++mm) {
            const float r0 = __builtin_amdgcn_rcpf(den[0][mm].x + 1e-6f), r1 = __builtin_amdgcn_rcpf(den[1][mm].x + 1e-6f);
            of[hp][mm] = v4u{pk2(num[0][mm].x * r0, num[0][mm].y * r0), pk2(num[0][mm].z * r0, num[0][mm].w * r0),
                             pk2(num[1][mm].x * r1, num[1][mm].y * r1), pk2(num[1][mm].z * r1, num[1][mm].w * r1)};
          }
          SB();
        }
        SB();
        if (X1_POS == 0 && hf == 1) x_issue(xs, rs, OFF_X1 + 2 * l * XBYTES + sx_, OFF_ST1 + 2 * l * STBYTES + ss_);
        if (ROLE_ORD && G == 1 && W1_G1) {
          if (hf == 1) {
#pragma unroll
            for (int u = 8; u < 16; ++u) { w1[u] = LD1N(u); w1[16 + u] = LD1N(16 + u); }
          }
        } else {
#pragma unroll
          for (int u = 8 + 4 * hf; u < 12 + 4 * hf; ++u) { w1[u] = LD1N(u); w1[16 + u] = LD1N(16 + u); }
        }
        LAS f32x4* red = (LAS f32x4*)(L + hf * LDS_REG + w * 8 * KB + l16);
#pragma unroll
        for (int nt = 0; nt < 4; ++nt) {
#pragma unroll
          for (int mm = 0; mm < 2; ++mm) {
            f32x4 acc = mfma32(wo[nt * 2], of[0][mm], zero);
            acc = mfma32(wo[nt * 2 + 1], of[1][mm], acc);
            red[(nt * 2 + mm) * 64] = acc;
          }
        }
        __syncthreads();
        if (X1_POS == 1 && hf == 1) x_issue(xs, rs, OFF_X1 + 2 * l * XBYTES + sx_, OFF_ST1 + 2 * l * STBYTES + ss_);
        red_out<false>(rsk, rsk, L, hf, x1o + hf * 32 * KB, s1o + hf * 4 * KB, g, c, hf, w);
        if (X1_POS == 2 && hf == 1) x_issue(xs, rs, OFF_X1 + 2 * l * XBYTES + sx_, OFF_ST1 + 2 * l * STBYTES + ss_);
      }
    }
    STAMP(3 + 6 * l);
    v4u hfr[14];
    int sh_ = OFF_H + l * HBYTES + ((g * 2 * 56 + 7 * w) * 2) * KB; OPQ(sh_);
    {
      const int xo = OFF_X1 + 2 * l * XBYTES + sx_, so = OFF_ST1 + 2 * l * STBYTES + ss_;
      if (l == 0) {
        f32x4 ca = zero, cb = zero;
#pragma unroll
        for (int u = 0; u < 16; ++u) { ca = mfma32(w1[u], ones4, ca); cb = mfma32(w1[16 + u], ones4, cb); }
        const int ln = fresh_lane();
        if ((ln & 15) == 0) { *(LAS f32x4*)(csl + 192 + 32 * w + 4 * (ln >> 4)) = ca; *(LAS f32x4*)(csl + 192 + 32 * w + 16 + 4 * (ln >> 4)) = cb; }
      }
#pragma unroll
      for (int hf = 0; hf < 2; ++hf) {
        x_finish(xs, rs, ws, xo + hf * 32 * KB, so + hf * 4 * KB, L, hf, w);
        const int lane = fresh_lane(), l16 = lane * 16, j = lane & 15, h = lane >> 4;
        const LAS v4u* xl = (const LAS v4u*)(L + hf * LDS_REG + l16);
        const LAS float* st = (const LAS float*)(L + LDS_STAT + hf * 256);
        f32x4 a0[2] = {zero, zero}, a1[2] = {zero, zero};
        v4u xr[3][2];
#pragma unroll
        for (int u = 0; u < 3; ++u) { xr[u][0] = xl[(u * 2) * 64]; xr[u][1] = xl[(u * 2 + 1) * 64]; }
        constexpr int W2S = W2_EARLY + ((ROLE_ORD && G == 1) ? W2X : 0);
        if (ROLE_ORD && G == 1 && hf == 0) {
#pragma unroll
          for (int i = 0; i < W2_EARLY; ++i) w2[i] = LD2(i);
        }
        if (ROLE_ORD && G == 1 && hf == 1) {
#pragma unroll
          for (int i = W2_EARLY; i < W2S; ++i) w2[i] = LD2(i);
        }
        SB();
#pragma unroll
        for (int u = 0; u < 16; ++u) {
          const v4u x0 = xr[u % 3][0], x1 = xr[u % 3][1];
          a0[0] = mfma32(w1[u], x0, a0[0]); a1[0] = mfma32(w1[16 + u], x0, a1[0]);
          a0[1] = mfma32(w1[u], x1, a0[1]); a1[1] = mfma32(w1[16 + u], x1, a1[1]);
          if (u + 3 < 16) { xr[u % 3][0] = xl[((u + 3) * 2) * 64]; xr[u % 3][1] = xl[((u + 3) * 2 + 1) * 64]; }
          if (hf == 0) { if (!ROLE_ORD && (u & 1) == 0 && W2_WO + (u >> 1) < W2_EARLY) w2[W2_WO + (u >> 1)] = LD2(W2_WO + (u >> 1)); }
          else { const int i0 = W2S + ((28 - W2S) * u) / 16, i1 = W2S + ((28 - W2S) * (u + 1)) / 16;
#pragma unroll
                 for (int i = i0; i < i1; ++i) w2[i] = LD2(i); }
          if (hf == 0 && u == XI_U) x_issue(xs, rs, xo + 32 * KB, so + 4 * KB);
          SB();
        }
        if (hf == 0 && XI_U == 16) x_issue(xs, rs, xo + 32 * KB, so + 4 * KB);
        if (ROLE_ORD && G == 0 && hf == 0) {
#pragma unroll
          for (int i = 0; i < W2_EARLY; ++i) w2[i] = LD2(i);
        }
        if (hf == 1) {
#pragma unroll
          for (int i = 0; i < 14; ++i) hfr[i] = LDH(i);
        }
        SB();
        {
          const f32x4 c0 = *(const LAS f32x4*)(csl + 192 + 32 * w + 4 * h), c1 = *(const LAS f32x4*)(csl + 192 + 32 * w + 16 + 4 * h);
#pragma unroll
          for (int mm = 0; mm < 2; ++mm) {
            const float mean = st[16 * mm + j], rstd = st[32 + 16 * mm + j];
            float t[8];
#pragma unroll
            for (int r = 0; r < 4; ++r) { t[r] = (a0[mm][r] - mean * c0[r]) * rstd; t[4 + r] = (a1[mm][r] - mean * c1[r]) * rstd; }
#pragma unroll
            for (int r = 0; r < 8; ++r) t[r] = t[r] * __builtin_amdgcn_rcpf(1.f + fexp(-t[r]));
            st128c(rs1, v4u{pk2s(t[0], t[1]), pk2s(t[2], t[3]), pk2s(t[4], t[5]), pk2s(t[6], t[7])}, l16,
                   OFF_H + l * HBYTES + (((g * 2 + hf) * 56 + 7 * c + w) * 2 + mm) * KB);
          }
        }
        if (FILL_MAIN && G == 0 && hf == 0) fill_group(__builtin_amdgcn_make_buffer_rsrc((void*)ws, 0, l == 0 ? WS_TOTAL : 0, 0x00020000), 1);
        SB();
      }
    }
    STAMP(4 + 6 * l);
    {
      const rsrc_t rsn = __builtin_amdgcn_make_buffer_rsrc((void*)ws, 0, l == 0 ? WS_TOTAL : 0, 0x00020000);
      const rsrc_t rsx = __builtin_amdgcn_make_buffer_rsrc((void*)ws, 0, (l == 0 && w < 4) ? WS_TOTAL : 0, 0x00020000);
      const rsrc_t rso = __builtin_amdgcn_make_buffer_rsrc((void*)out, 0, (l == 1 && w < 4) ? S * D * 2 : 0, 0x00020000);
      const int x2o = OFF_X1 + XBYTES + g * 64 * KB, s2o = OFF_ST1 + STBYTES + g * 8 * KB;
#pragma unroll
      for (int hf = 0; hf < 2; ++hf) {
        const int l16 = fresh_lane() * 16;
        if (__builtin_expect(!valid(hmax(hfr)), 0)) {
          unsigned n = 0;
          do {
            if (spin_fail(n, rs)) break;
            unsigned vb = (unsigned)(fresh_lane() * 16 + sh_ + hf * 112 * KB);
#pragma unroll
            for (int i = 0; i < 14; ++i) { reld128(hfr[i], ws, vb); vb += KB; asm volatile("" : "+v"(vb)); }
            asm volatile("s_waitcnt vmcnt(0)" : "+v"(hfr[0]), "+v"(hfr[1]), "+v"(hfr[2]), "+v"(hfr[3]), "+v"(hfr[4]), "+v"(hfr[5]), "+v"(hfr[6]),
                         "+v"(hfr[7]), "+v"(hfr[8]), "+v"(hfr[9]), "+v"(hfr[10]), "+v"(hfr[11]), "+v"(hfr[12]), "+v"(hfr[13]) :: "memory");
          } while (!valid(hmax(hfr)));
        }
        if (hf == 0) STAMP(5 + 6 * l);
        f32x4 acc[4][2];
#pragma unroll
        for (int nt = 0; nt < 4; ++nt) { acc[nt][0] = zero; acc[nt][1] = zero; }
#pragma unroll
        for (int u = 0; u < 7; ++u) {
#pragma unroll
          for (int nt = 0; nt < 4; ++nt) {
            acc[nt][0] = mfma32(w2[u * 4 + nt], hfr[u * 2], acc[nt][0]);
            acc[nt][1] = mfma32(w2[u * 4 + nt], hfr[u * 2 + 1], acc[nt][1]);
          }
          if (hf == 1) {
#pragma unroll
            for (int i = (WKV_LOOP * u) / 7; i < (WKV_LOOP * (u + 1)) / 7; ++i) wkv[i] = LDK(rsn, i);
          }
          SB();
        }
        if (hf == 0) {
        } else {
          x_issue(xs, rsn, OFF_X1 + XBYTES + sx_, OFF_ST1 + STBYTES + ss_);
        }
        red_write(acc, L, hf, w);
        __syncthreads();
        if (hf == 0) {
#pragma unroll
          for (int i = 0; i < 14; ++i) hfr[i] = LDH(112 + i);
        }
        red_out<true>(rsx, rso, L, hf, x2o + hf * 32 * KB, s2o + hf * 4 * KB, g, c, hf, w);
        if (hf == 1) {
#pragma unroll
          for (int i = WKV_LOOP; i < 16; ++i) wkv[i] = LDK(rsn, i);
        }
      }
    }
    STAMP(6 + 6 * l);
  }
  STAMP_FLUSH;
}
__global__ __launch_bounds__(512, 2) void k_main(unsigned char* ws, const uint16_t* xin, uint16_t* out) {
  extern __shared__ __attribute__((aligned(16))) unsigned char smem[];
  LAS unsigned char* L = (LAS unsigned char*)smem;
  if (__builtin_amdgcn_readfirstlane(threadIdx.x >> 6) < 4) run<0>(ws, xin, out, L);
  else run<1>(ws, xin, out, L);
}
}

extern "C" void kernel_launch(void* const* d_in, const int* in_sizes, int n_in, void* d_out, int out_size, void* d_ws, size_t ws_size, hipStream_t stream) {
  const uint16_t* x = (const uint16_t*)d_in[0];
  const uint16_t* wqkv = (const uint16_t*)d_in[1];
  const uint16_t* wo = (const uint16_t*)d_in[2];
  const uint16_t* w1 = (const uint16_t*)d_in[4];
  const uint16_t* w2 = (const uint16_t*)d_in[5];
  unsigned char* ws = (unsigned char*)d_ws;
  uint16_t* out = (uint16_t*)d_out;
  (void)hipFuncSetAttribute((const void*)k_main, hipFuncAttributeMaxDynamicSharedMemorySize, LDS_BYTES);
  k_prep<<<PREP_BLOCKS, 256, 0, stream>>>(wqkv, wo, w1, w2, ws);
  k_main<<<256, 512, LDS_BYTES, stream>>>(ws, x, out);
}
```

```cpp
#include <hip/hip_runtime.h>
#include <hip/hip_bf16.h>
#include <stdint.h>

#define GAS __attribute__((address_space(1)))
#define LAS __attribute__((address_space(3)))
typedef unsigned v4u __attribute__((ext_vector_type(4)));
typedef unsigned v2u __attribute__((ext_vector_type(2)));
typedef float f32x4 __attribute__((ext_vector_type(4)));
typedef float f32x2 __attribute__((ext_vector_type(2)));
typedef __bf16 bf16x8 __attribute__((ext_vector_type(8)));
typedef __bf16 bf16x2 __attribute__((ext_vector_type(2)));
typedef short s16x4 __attribute__((ext_vector_type(4)));

#define STAMP(slot)
#define STAMP_DECL
#define STAMP_FLUSH

namespace {
constexpr int S = 2048, D = 512, F = 1792, H = 32;
constexpr int NG = 32;
constexpr int KB = 1024;
constexpr unsigned SENT = 0xFFFFFFFFu;
constexpr int OFF_WQKV = 0;
constexpr int OFF_WO = OFF_WQKV + 96 * 16 * KB;
constexpr int OFF_W1 = OFF_WO + 32 * 16 * KB;
constexpr int OFF_W2 = OFF_W1 + 112 * 16 * KB;
constexpr int OFF_CSQ = OFF_W2 + 32 * 56 * KB;
constexpr int OFF_CS1 = OFF_CSQ + 8 * KB;
constexpr int OFF_TMO = OFF_CS1 + 8 * KB;
constexpr int XBYTES = NG * 64 * KB;
constexpr int STBYTES = NG * 8 * KB;
constexpr int OFF_X0 = OFF_TMO + 4 * KB;
constexpr int OFF_ST0 = OFF_X0 + XBYTES;
constexpr int OFF_FILL0 = OFF_ST0 + STBYTES;
constexpr int KVPBYTES = NG * H * KB;
constexpr int OFF_KVP = OFF_FILL0;
constexpr int ZPBYTES = NG * H * 64;
constexpr int OFF_ZP = OFF_KVP + 2 * KVPBYTES;
constexpr int KVRBYTES = 16 * KB;
constexpr int OFF_KVR = OFF_ZP + 2 * ZPBYTES;
constexpr int ZRBYTES = KB;
constexpr int OFF_ZR = OFF_KVR + 2 * KVRBYTES;
constexpr int OFF_ST1 = OFF_ZR + 2 * ZRBYTES;
constexpr int OFF_PQ0 = OFF_ST1 + 3 * STBYTES;
constexpr int OFF_X1 = OFF_PQ0 + XBYTES;
#define FILL_MAIN 1
constexpr int OFF_PQ1 = OFF_X1 + 3 * XBYTES;
constexpr int HBYTES = NG * 224 * KB;
constexpr int OFF_H = OFF_PQ1 + XBYTES;
constexpr int WS_TOTAL = OFF_H + 2 * HBYTES;
constexpr int OFF_FILL1 = FILL_MAIN ? OFF_X1 + XBYTES : WS_TOTAL;
static_assert((OFF_FILL0 % 1024) == 0 && (OFF_FILL1 % 1024) == 0, "");
__host__ __device__ constexpr int xoff(int xi) { return xi == 0 ? OFF_X0 : OFF_X1 + (xi - 1) * XBYTES; }
__host__ __device__ constexpr int stoff(int xi) { return xi == 0 ? OFF_ST0 : OFF_ST1 + (xi - 1) * STBYTES; }

__device__ __forceinline__ f32x4 mfma32(v4u a, v4u b, f32x4 c) {
  return __builtin_amdgcn_mfma_f32_16x16x32_bf16(__builtin_bit_cast(bf16x8, a), __builtin_bit_cast(bf16x8, b), c, 0, 0, 0);
}
__device__ __forceinline__ f32x4 mfma16(v2u a, v2u b, f32x4 c) {
  return __builtin_amdgcn_mfma_f32_16x16x16bf16_1k(__builtin_bit_cast(s16x4, a), __builtin_bit_cast(s16x4, b), c, 0, 0, 0);
}
__device__ __forceinline__ unsigned pk2(float lo, float hi) {
  bf16x2 v; v.x = (__bf16)lo; v.y = (__bf16)hi;
  return __builtin_bit_cast(unsigned, v);
}
__device__ __forceinline__ unsigned san(unsigned u) { return u < 0xFFFFFFFEu ? u : 0xFFFFFFFEu; }
__device__ __forceinline__ unsigned pk2s(float lo, float hi) { return san(pk2(lo, hi)); }
__device__ __forceinline__ float asf(unsigned u) { return __builtin_bit_cast(float, u); }
__device__ __forceinline__ float bflo(unsigned u) { return __builtin_bit_cast(float, u << 16); }
__device__ __forceinline__ float bfhi(unsigned u) { return __builtin_bit_cast(float, u & 0xFFFF0000u); }
__device__ __forceinline__ float fexp(float t) { return __builtin_amdgcn_exp2f(t * 1.44269504088896f); }
__device__ __forceinline__ float phi(float t) { return (t > 0.f ? t + 1.f : fexp(t)) + 1e-6f; }
__device__ __forceinline__ int permk(int h, int i) { return i < 4 ? 4 * h + i : 16 + 4 * h + (i - 4); }
#define XMAP 2
__device__ __forceinline__ void blk_gc(int b, int& g, int& c) {
  const int xcd = b & 7, idx = b >> 3;
  if (XMAP == 1) { g = xcd * 4 + (idx >> 3); c = idx & 7; }
  else if (XMAP == 2) { g = (xcd >> 1) * 8 + (idx >> 2); c = (xcd & 1) * 4 + (idx & 3); }
  else { g = (xcd >> 2) * 16 + (idx >> 1); c = (xcd & 3) * 2 + (idx & 1); }
}
__device__ __forceinline__ unsigned umax(unsigned a, unsigned b) { return a > b ? a : b; }
__device__ __forceinline__ unsigned umax4(v4u v) { return umax(umax(v.x, v.y), umax(v.z, v.w)); }
#define SB() __builtin_amdgcn_sched_barrier(0)

constexpr int PREP_BLOCKS = 352;
__global__ __launch_bounds__(256) void k_prep(const uint16_t* __restrict__ wqkv, const uint16_t* __restrict__ wo,
                                              const uint16_t* __restrict__ w1, const uint16_t* __restrict__ w2, unsigned char* __restrict__ ws) {
  __shared__ __attribute__((aligned(16))) unsigned char sm[4 * 32 * 144];
  const int tid = threadIdx.x, lane = tid & 63, w = __builtin_amdgcn_readfirstlane(tid >> 6);
  int id = blockIdx.x * 4 + w;
  const uint16_t* W; int N, off, UT, u, ng;
  if (id < 384) { W = wqkv; N = 1536; off = OFF_WQKV; UT = 16; u = id & 15; ng = id >> 4; }
  else if (id < 512) { id -= 384; W = wo; N = 512; off = OFF_WO; UT = 16; u = id & 15; ng = id >> 4; }
  else if (id < 960) { id -= 512; W = w1; N = 1792; off = OFF_W1; UT = 16; u = id & 15; ng = id >> 4; }
  else { id -= 960; W = w2; N = 512; off = OFF_W2; UT = 56; u = id % 56; ng = id / 56; }
  v4u v[4];
#pragma unroll
  for (int q = 0; q < 4; ++q) v[q] = *(const v4u*)(W + (size_t)(32 * u + 8 * q + (lane >> 3)) * N + ng * 64 + (lane & 7) * 8);
  {
    const int n16 = (OFF_FILL1 - OFF_FILL0) / 16;
    v4u* p = (v4u*)(ws + OFF_FILL0);
    for (int i = blockIdx.x * 256 + tid; i < n16; i += PREP_BLOCKS * 256) p[i] = v4u{SENT, SENT, SENT, SENT};
    if (blockIdx.x == 0 && tid == 0) *(unsigned*)(ws + OFF_TMO) = 0u;
  }
  LAS unsigned char* S = (LAS unsigned char*)sm + w * (32 * 144);
#pragma unroll
  for (int q = 0; q < 4; ++q) *(LAS v4u*)(S + (8 * q + (lane >> 3)) * 144 + (lane & 7) * 16) = v[q];
  const int h = lane >> 4, qq = (lane & 15) >> 2, p = lane & 3;
#pragma unroll
  for (int t = 0; t < 4; ++t) {
    const s16x4 lo = __builtin_amdgcn_ds_read_tr16_b64_v4i16((LAS s16x4*)(S + (4 * h + qq) * 144 + (16 * t + 4 * p) * 2));
    const s16x4 hi = __builtin_amdgcn_ds_read_tr16_b64_v4i16((LAS s16x4*)(S + (16 + 4 * h + qq) * 144 + (16 * t + 4 * p) * 2));
    const v2u a = __builtin_bit_cast(v2u, lo), b = __builtin_bit_cast(v2u, hi);
    *(v4u*)(ws + off + ((size_t)((ng * 4 + t) * UT + u) * 64 + lane) * 16) = v4u{a.x, a.y, b.x, b.y};
  }
}

typedef __amdgpu_buffer_rsrc_t rsrc_t;
constexpr int SC1 = 16;
__device__ __forceinline__ v4u ld128(rsrc_t rs, int voff, int soff) { return __builtin_amdgcn_raw_buffer_load_b128(rs, voff, soff, 0); }
__device__ __forceinline__ v4u ld128c(rsrc_t rs, int voff, int soff) { return __builtin_amdgcn_raw_buffer_load_b128(rs, voff, soff, SC1); }
__device__ __forceinline__ v2u ld64c(rsrc_t rs, int voff, int soff) { return __builtin_amdgcn_raw_buffer_load_b64(rs, voff, soff, SC1); }
__device__ __forceinline__ unsigned ld32c(rsrc_t rs, int voff, int soff) { return __builtin_amdgcn_raw_buffer_load_b32(rs, voff, soff, SC1); }
__device__ __forceinline__ void st128c(rsrc_t rs, v4u v, int voff, int soff) { __builtin_amdgcn_raw_buffer_store_b128(v, rs, (int)((unsigned)voff + (unsigned)soff), 0, SC1); }
__device__ __forceinline__ void st64c(rsrc_t rs, v2u v, int voff, int soff) { __builtin_amdgcn_raw_buffer_store_b64(v, rs, voff, soff, SC1); }
__device__ __forceinline__ void st32c(rsrc_t rs, unsigned v, int voff, int soff) { __builtin_amdgcn_raw_buffer_store_b32(v, rs, voff, soff, SC1); }
__device__ __forceinline__ bool valid(unsigned m) { return __builtin_amdgcn_ballot_w64(m == SENT) == 0ull; }
__device__ __forceinline__ unsigned asu(float f) { return __builtin_bit_cast(unsigned, f); }
template <int N> __device__ __forceinline__ float ror_add(float x) {
  return x + asf((unsigned)__builtin_amdgcn_update_dpp(0, (int)asu(x), 0x120 | N, 0xF, 0xF, false));
}
__device__ __forceinline__ float sum_x16(float x) { auto r = __builtin_amdgcn_permlane16_swap(asu(x), asu(x), false, false); return asf(r[0]) + asf(r[1]); }
__device__ __forceinline__ float sum_x32(float x) { auto r = __builtin_amdgcn_permlane32_swap(asu(x), asu(x), false, false); return asf(r[0]) + asf(r[1]); }
constexpr unsigned SPIN_CAP = 1u << 14;
__device__ __forceinline__ bool spin_fail(unsigned& n, rsrc_t rs) {
  asm volatile("" ::: "memory");
  __builtin_amdgcn_s_sleep(2);
  ++n;
  if (n > SPIN_CAP) { st32c(rs, 1u, OFF_TMO, 0); return true; }
  if ((n & 63u) == 0u) { if (__builtin_amdgcn_readfirstlane(ld32c(rs, OFF_TMO, 0)) != 0) return true; }
  return false;
}

#define DUTY_POS 0
#define W1_G 2
#define POLL_K0 2
#define PQ_K 3
#define WKV_LOOP 10
#define PQ_SLEEP 32
#define ROLE_ORD 1
#define W2X 6
#define WO_G1 1
#define W1_G1 0
#define FILL_HF 0
#define CODE_PF 48
#define PQ_POS 1
#define X1_POS 2
#define WQQ_POS 2
#define H1_POS 1
#define H1_U 3
#define W1_Q 6
#define XI_U 16
#define W1_EARLY 14
#define KVR_SLEEP 4
#define W1_MID 6
#define W2_EARLY 8
#define W2_WO 0
constexpr int LDS_REG = 64 * KB;
constexpr int LDS_STAT = 2 * LDS_REG;
constexpr int LDS_CS = LDS_STAT + 512;
constexpr int LDS_RES = LDS_CS + 2048;
constexpr int LDS_VX = LDS_RES + 8 * KB;
constexpr int LDS_KV = LDS_VX;
constexpr int LDS_BYTES = LDS_VX + 17 * KB;
constexpr int OOBV = 0x7FFF0000;

__device__ __forceinline__ int fresh_lane() {
  unsigned z;
  asm volatile("s_mov_b32 %0, 0" : "=s"(z));
  return (int)__builtin_amdgcn_mbcnt_hi(~0u, __builtin_amdgcn_mbcnt_lo(~0u, z));
}
#define OPQ(x) asm volatile("" : "+s"(x))

__device__ __forceinline__ unsigned hmax(const v4u (&a)[14]) {
  unsigned m = 0;
#pragma unroll
  for (int i = 0; i < 14; ++i) m = umax(m, umax4(a[i]));
  return m;
}
__device__ __forceinline__ void reld128(v4u& a, const unsigned char* base, unsigned off) {
  asm volatile("s_nop 4\n\tglobal_load_dwordx4 %0, %1, %2 sc1" : "+v"(a) : "v"(off), "s"(base) : "memory");
}
__device__ __forceinline__ void reld64(v2u& a, const unsigned char* base, unsigned off) {
  asm volatile("s_nop 4\n\tglobal_load_dwordx2 %0, %1, %2 sc1" : "+v"(a) : "v"(off), "s"(base) : "memory");
}
struct XS { v4u x[4]; v2u s; };
__device__ __forceinline__ void x_issue(XS& a, rsrc_t rs, int xo, int so) {
  const int ln = fresh_lane();
#pragma unroll
  for (int i = 0; i < 4; ++i) a.x[i] = ld128c(rs, ln * 16 + i * KB, xo);
  a.s = ld64c(rs, ln * 8, so);
}
__device__ __forceinline__ unsigned x_max(const XS& a) { return umax(umax(umax4(a.x[0]), umax4(a.x[1])), umax(umax(umax4(a.x[2]), umax4(a.x[3])), umax(a.s.x, a.s.y))); }
__device__ __forceinline__ void x_finish(XS& a, rsrc_t rs, const unsigned char* ws, int xo, int so, LAS unsigned char* L, int par, int w) {
  if (__builtin_expect(!valid(x_max(a)), 0)) {
    unsigned n = 0;
    do {
      if (spin_fail(n, rs)) break;
      const unsigned ln = (unsigned)fresh_lane();
#pragma unroll
      for (int i = 0; i < 4; ++i) reld128(a.x[i], ws, ln * 16 + xo + i * KB);
      reld64(a.s, ws, ln * 8 + so);
      asm volatile("s_waitcnt vmcnt(0)" : "+v"(a.x[0]), "+v"(a.x[1]), "+v"(a.x[2]), "+v"(a.x[3]), "+v"(a.s) :: "memory");
    } while (!valid(x_max(a)));
  }
  const int ln = fresh_lane();
  LAS unsigned char* dst = L + par * LDS_REG + w * 4096 + ln * 16;
#pragma unroll
  for (int i = 0; i < 4; ++i) *(LAS v4u*)(dst + i * KB) = a.x[i];
  const float mp = asf(a.s.x), m2p = asf(a.s.y);
  const float mean = ror_add<1>(ror_add<2>(ror_add<4>(ror_add<8>(mp)))) * (1.f / 16.f);
  const float dm = mp - mean;
  const float m2 = ror_add<1>(ror_add<2>(ror_add<4>(ror_add<8>(m2p + 32.f * dm * dm))));
  const float rstd = __builtin_amdgcn_rsqf(m2 * (1.f / 512.f) + 1e-5f);
  if ((ln & 15) == 0) {
    LAS float* st = (LAS float*)(L + LDS_STAT + par * 256) + w * 4 + (ln >> 4);
    st[0] = mean; st[32] = rstd;
  }
  __syncthreads();
}

__device__ __forceinline__ void xraw_issue(XS& a, rsrc_t rx, int row0) {
  const int ln = fresh_lane();
#pragma unroll
  for (int i = 0; i < 4; ++i) a.x[i] = ld128(rx, ln * 16 + i * KB, row0 * KB);
}
__device__ __forceinline__ float wave_sum(float x) { return sum_x32(sum_x16(ror_add<1>(ror_add<2>(ror_add<4>(ror_add<8>(x)))))); }
__device__ __forceinline__ void xraw_finish(XS& a, LAS unsigned char* L, int par, int w) {
  const int ln = fresh_lane();
  const int u = ln >> 2, q = ln & 3;
#pragma unroll
  for (int i = 0; i < 4; ++i) {
    const int tok = 4 * w + i;
    LAS unsigned char* d = L + par * LDS_REG + (u * 2 + (tok >> 4)) * KB + ((tok & 15) + 32 * (q & 1)) * 16 + (q >> 1) * 8;
    *(LAS v2u*)d = v2u{a.x[i].x, a.x[i].y};
    *(LAS v2u*)(d + 256) = v2u{a.x[i].z, a.x[i].w};
    const float v[8] = {bflo(a.x[i].x), bfhi(a.x[i].x), bflo(a.x[i].y), bfhi(a.x[i].y), bflo(a.x[i].z), bfhi(a.x[i].z), bflo(a.x[i].w), bfhi(a.x[i].w)};
    const float mean = wave_sum(((v[0] + v[1]) + (v[2] + v[3])) + ((v[4] + v[5]) + (v[6] + v[7]))) * (1.f / 512.f);
    float m2 = 0.f;
#pragma unroll
    for (int e = 0; e < 8; ++e) { const float dd = v[e] - mean; m2 += dd * dd; }
    m2 = wave_sum(m2);
    if (ln == 0) {
      LAS float* st = (LAS float*)(L + LDS_STAT + par * 256) + tok;
      st[0] = mean; st[32] = __builtin_amdgcn_rsqf(m2 * (1.f / 512.f) + 1e-5f);
    }
  }
  __syncthreads();
}

__device__ __forceinline__ void red_write(f32x4 (&acc)[4][2], LAS unsigned char* L, int par, int w) {
  const int ln = fresh_lane();
  LAS f32x4* red = (LAS f32x4*)(L + par * LDS_REG + w * 8 * KB + ln * 16);
#pragma unroll
  for (int nt = 0; nt < 4; ++nt)
#pragma unroll
    for (int mm = 0; mm < 2; ++mm) red[(nt * 2 + mm) * 64] = acc[nt][mm];
}
template <bool LAST>
__device__ __forceinline__ void red_out(rsrc_t rsw, rsrc_t rso, LAS unsigned char* L, int par, int xo, int so, int g, int c, int hf, int w) {
  const int ln = fresh_lane();
  const int mm = (w >> 1) & 1, uu = w & 1;
  const int j = ln & 15, h = ln >> 4;
  v4u o = {0, 0, 0, 0};
  v2u stv = {0, 0};
  if (w < 4) {
    const LAS f32x4* red = (const LAS f32x4*)(L + par * LDS_REG + (4 * uu + mm) * KB + ln * 16);
    LAS v4u* resp = (LAS v4u*)(L + LDS_RES + (hf * 4 + w) * KB + ln * 16);
    const v4u resid = *resp;
    f32x4 s0 = {0, 0, 0, 0}, s1 = {0, 0, 0, 0};
#pragma unroll
    for (int ww = 0; ww < 8; ++ww) {
      s0 += red[ww * 512];
      s1 += red[ww * 512 + 128];
    }
    s0.x += bflo(resid.x); s0.y += bfhi(resid.x); s0.z += bflo(resid.y); s0.w += bfhi(resid.y);
    s1.x += bflo(resid.z); s1.y += bfhi(resid.z); s1.z += bflo(resid.w); s1.w += bfhi(resid.w);
    o = v4u{pk2s(s0.x, s0.y), pk2s(s0.z, s0.w), pk2s(s1.x, s1.y), pk2s(s1.z, s1.w)};
    *resp = o;
  }
  asm volatile("" : "+v"(o));
  st128c(rsw, o, ln * 16, xo + ((2 * c + uu) * 2 + mm) * KB);
  if constexpr (LAST) {
    const int voff = j * (D * 2) + h * 8, soff = ((g * 64 + hf * 32 + 16 * mm) * D + 64 * c + 32 * uu) * 2;
    __builtin_amdgcn_raw_buffer_store_b64(v2u{o.x, o.y}, rso, voff, soff, 0);
    __builtin_amdgcn_raw_buffer_store_b64(v2u{o.z, o.w}, rso, voff, soff + 32, 0);
  }
  if (w < 4) {
    float v[8] = {bflo(o.x), bfhi(o.x), bflo(o.y), bfhi(o.y), bflo(o.z), bfhi(o.z), bflo(o.w), bfhi(o.w)};
    const float s = sum_x32(sum_x16(((v[0] + v[1]) + (v[2] + v[3])) + ((v[4] + v[5]) + (v[6] + v[7]))));
    const float mean = s * (1.f / 32.f);
    float m2 = 0.f;
#pragma unroll
    for (int i = 0; i < 8; ++i) { float d = v[i] - mean; m2 += d * d; }
    m2 = sum_x32(sum_x16(m2));
    stv = v2u{san(asu(mean)), san(asu(m2))};
  }
  asm volatile("" : "+v"(stv));
  st64c(rsw, stv, h == 0 ? j * 128 : OOBV, so + (16 * mm * 16 + 2 * c + uu) * 8);
}

template <int G>
__device__ __forceinline__ void run(unsigned char* ws, const uint16_t* xin, uint16_t* out, LAS unsigned char* L) {
  const int w = G * 4 + (__builtin_amdgcn_readfirstlane(threadIdx.x >> 6) & 3);
  int g, c; blk_gc(blockIdx.x, g, c);
  const rsrc_t rs = __builtin_amdgcn_make_buffer_rsrc((void*)ws, 0, WS_TOTAL, 0x00020000);
  const rsrc_t rx = __builtin_amdgcn_make_buffer_rsrc((void*)xin, 0, S * D * 2, 0x00020000);
  const rsrc_t rsnull = __builtin_amdgcn_make_buffer_rsrc((void*)ws, 0, 0, 0x00020000);
  const f32x4 zero = {0, 0, 0, 0};
  const int hd = 4 * c + (w & 3);
  int skv = OFF_WQKV + ((w < 4 ? 32 : 64) + hd) * 16 * KB;
  int sq_ = OFF_WQKV + hd * 16 * KB;
  int so_ = OFF_WO + (4 * c * 16 + 2 * w) * KB;
  int s1_ = OFF_W1 + (14 * c + 2 * (w < 7 ? w : 6)) * 16 * KB;
  int s2_ = OFF_W2 + (4 * c * 56 + 7 * w) * KB;
  int sx_ = w * 4096 + g * 64 * KB;
  int ss_ = w * 512 + g * 8 * KB;
  const rsrc_t rs1 = __builtin_amdgcn_make_buffer_rsrc((void*)ws, 0, w < 7 ? WS_TOTAL : 0, 0x00020000);
  const rsrc_t rsk = __builtin_amdgcn_make_buffer_rsrc((void*)ws, 0, w < 4 ? WS_TOTAL : 0, 0x00020000);
  STAMP_DECL
  STAMP(0);
#define LDO(i) ld128(rs, l16 + ((i) & 1) * KB, so_ + ((i) >> 1) * 16 * KB)
#define LD1(i) ld128(rs, l16 + ((i) & 3) * KB, s1_ + ((i) >> 2) * 4 * KB)
#define LD1N(i) ld128(rs1, l16 + ((i) & 3) * KB, s1_ + ((i) >> 2) * 4 * KB)
#define LD2(i) ld128(rs, l16 + (((i) >> 2) & 3) * KB, s2_ + (((i) & 3) * 56 + (((i) >> 2) & 4)) * KB)
#define LDK(r_, i) ld128(r_, l16 + ((i) & 3) * KB, skv + ((i) >> 2) * 4 * KB)
#define LDH(i) ld128c(rs, l16 + ((i) & 3) * KB, sh_ + ((i) >> 2) * 4 * KB)
  v4u wkv[16];
  XS xs;
  {
    const int lane = fresh_lane(), l16 = lane * 16;
    xraw_issue(xs, rx, g * 64 + 4 * w);
#pragma unroll
    for (int i = 0; i < 16; ++i) wkv[i] = LDK(rs, i);
    if (w < 4) {
#pragma unroll
      for (int hf = 0; hf < 2; ++hf) {
        const int vo_ = (lane & 15) * KB + (lane >> 4) * 8, so2 = (g * 64 + hf * 32 + 16 * (w >> 1)) * KB + (64 * c + 32 * (w & 1)) * 2;
        const v2u a = __builtin_amdgcn_raw_buffer_load_b64(rx, vo_, so2, 0), b = __builtin_amdgcn_raw_buffer_load_b64(rx, vo_ + 32, so2, 0);
        *(LAS v4u*)(L + LDS_RES + (hf * 4 + w) * KB + l16) = v4u{a.x, a.y, b.x, b.y};
      }
    }
  }
  LAS float* csl = (LAS float*)(L + LDS_CS);
  const v4u ones4 = {0x3F803F80u, 0x3F803F80u, 0x3F803F80u, 0x3F803F80u};
  auto fill_group = [&](rsrc_t rf, int layer) {
    const int l16 = fresh_lane() * 16;
    const v4u sv = {SENT, SENT, SENT, SENT};
#pragma unroll
    for (int i = 0; i < 7; ++i) {
      const int f = 7 * (w & 3) + i;
      st128c(rf, sv, l16, OFF_H + layer * HBYTES + (((g * 2 + f / 14) * 56 + 7 * c + (f % 14) / 2) * 2 + (f & 1)) * KB);
    }
#pragma unroll
    for (int hf = 0; hf < 2; ++hf) st128c(rf, sv, l16, OFF_X1 + (1 + layer) * XBYTES + (g * 2 + hf) * 32 * KB + ((2 * c + (w & 1)) * 2 + ((w >> 1) & 1)) * KB);
    if (layer == 0) {
#pragma unroll
      for (int hf = 0; hf < 2; ++hf) st128c(rf, sv, l16, OFF_PQ1 + ((g * 2 + hf) * H + hd) * KB);
    }
  };
  SB();
#pragma unroll 1
  for (int l = 0; l < 2; ++l) {
    v4u wqq[16], wo[8], w1[32], w2[28];
    OPQ(skv); OPQ(sq_); OPQ(so_); OPQ(s1_); OPQ(s2_); OPQ(sx_); OPQ(ss_);
    {
      const int xo = OFF_X1 + XBYTES + sx_, so = OFF_ST1 + STBYTES + ss_;
      v4u fr[2];
#pragma unroll
      for (int hf = 0; hf < 2; ++hf) {
        if (l == 0) {
          if (hf == 0) {
            f32x4 cs = zero;
#pragma unroll
            for (int u = 0; u < 16; ++u) cs = mfma32(ones4, wkv[u], cs);
            const int ln = fresh_lane();
            if (ln < 16) csl[64 + 16 * w + ln] = cs.x;
          }
          xraw_finish(xs, L, hf, w);
        } else x_finish(xs, rs, ws, xo + hf * 32 * KB, so + hf * 4 * KB, L, hf, w);
        const int lane = fresh_lane(), l16 = lane * 16, j = lane & 15, h = lane >> 4;
        const LAS v4u* xl = (const LAS v4u*)(L + hf * LDS_REG + l16);
        const LAS float* st = (const LAS float*)(L + LDS_STAT + hf * 256);
        v4u xr[4][2];
#pragma unroll
        for (int u = 0; u < 4; ++u) { xr[u][0] = xl[(u * 2) * 64]; xr[u][1] = xl[(u * 2 + 1) * 64]; }
        if (ROLE_ORD && G == 1) {
#pragma unroll
          for (int i = 0; i < 8; ++i) wqq[hf * 8 + i] = ld128(rs, l16 + (i & 3) * KB, sq_ + (hf * 8 + (i & 4)) * KB);
          if (WO_G1 == hf) {
#pragma unroll
            for (int i = 0; i < 8; ++i) wo[i] = LDO(i);
          }
        }
        SB();
        f32x4 a[2] = {zero, zero};
#pragma unroll
        for (int u = 0; u < 16; ++u) {
          a[0] = mfma32(xr[u % 4][0], wkv[u], a[0]);
          a[1] = mfma32(xr[u % 4][1], wkv[u], a[1]);
          if (u + 4 < 16) { xr[u % 4][0] = xl[((u + 4) * 2) * 64]; xr[u % 4][1] = xl[((u + 4) * 2 + 1) * 64]; }
          if (hf == 0 && u == XI_U) { if (l == 0) xraw_issue(xs, rx, g * 64 + 32 + 4 * w); else x_issue(xs, rs, xo + 32 * KB, so + 4 * KB); }
          if (WQQ_POS == 1 && (u & 1) == 1) wqq[hf * 8 + (u >> 1)] = ld128(rs, l16 + ((u >> 1) & 3) * KB, sq_ + (hf * 8 + ((u >> 1) & 4)) * KB);
          SB();
        }
        if (hf == 0 && XI_U == 16) { if (l == 0) xraw_issue(xs, rx, g * 64 + 32 + 4 * w); else x_issue(xs, rs, xo + 32 * KB, so + 4 * KB); }
        if (WQQ_POS == 2 && !(ROLE_ORD && G == 1)) {
#pragma unroll
          for (int i = 0; i < 8; ++i) wqq[hf * 8 + i] = ld128(rs, l16 + (i & 3) * KB, sq_ + (hf * 8 + (i & 4)) * KB);
        }
        if (hf == 1 && !(ROLE_ORD && G == 1)) {
#pragma unroll
          for (int i = 0; i < 8; ++i) wo[i] = LDO(i);
        }
        const float cs = csl[64 + 16 * w + j];
        unsigned f[4];
#pragma unroll
        for (int mm = 0; mm < 2; ++mm) {
          const f32x4 mean = *(const LAS f32x4*)(st + 16 * mm + 4 * h), rstd = *(const LAS f32x4*)(st + 32 + 16 * mm + 4 * h);
          float t[4];
#pragma unroll
          for (int r = 0; r < 4; ++r) { const float tt = (a[mm][r] - mean[r] * cs) * rstd[r]; t[r] = w < 4 ? phi(tt) : tt; }
          f[2 * mm] = pk2(t[0], t[1]); f[2 * mm + 1] = pk2(t[2], t[3]);
        }
        fr[hf] = v4u{f[0], f[1], f[2], f[3]};
        if (FILL_MAIN && G == 0 && hf == FILL_HF) fill_group(__builtin_amdgcn_make_buffer_rsrc((void*)ws, 0, l == 0 ? WS_TOTAL : 0, 0x00020000), 0);
      }
      const int lane = fresh_lane(), l16 = lane * 16, j = lane & 15, h = lane >> 4;
      LAS v4u* vx = (LAS v4u*)(L + LDS_VX + (w & 3) * 2 * KB + l16);
      if (w >= 4) { vx[0] = fr[0]; vx[64] = fr[1]; }
      __syncthreads();
      {
        f32x4 kv = zero, z = zero;
        if (w < 4) {
          kv = mfma32(fr[0], vx[0], kv); z = mfma32(fr[0], ones4, z);
          kv = mfma32(fr[1], vx[64], kv); z = mfma32(fr[1], ones4, z);
        }
        asm volatile("" : "+v"(kv), "+v"(z));
        const v4u kvu = __builtin_bit_cast(v4u, kv), zu = __builtin_bit_cast(v4u, z);
        st128c(rsk, v4u{san(kvu.x), san(kvu.y), san(kvu.z), san(kvu.w)}, l16, OFF_KVP + l * KVPBYTES + (g * H + hd) * KB);
        st128c(rsk, v4u{san(zu.x), san(zu.y), san(zu.z), san(zu.w)}, j == 0 ? h * 16 : OOBV, OFF_ZP + l * ZPBYTES + (g * H + hd) * 64);
      }
      SB();
      {
        const int hf = w >> 2;
        const LAS v4u* xl = (const LAS v4u*)(L + hf * LDS_REG + l16);
        const LAS float* st = (const LAS float*)(L + LDS_STAT + hf * 256);
        v4u xr[4][2];
#pragma unroll
        for (int u = 0; u < 4; ++u) { xr[u][0] = xl[(u * 2) * 64]; xr[u][1] = xl[(u * 2 + 1) * 64]; }
        SB();
        if (ROLE_ORD && G == 1 && WO_G1 == 2) {
#pragma unroll
          for (int i = 0; i < 8; ++i) wo[i] = LDO(i);
        }
        w1[0] = LD1N(0); w1[16] = LD1N(16); w1[1] = LD1N(1); w1[17] = LD1N(17);
        if (l == 0) {
          f32x4 cs = zero;
#pragma unroll
          for (int u = 0; u < 16; ++u) cs = mfma32(wqq[u], ones4, cs);
          if (j == 0) *(LAS f32x4*)(csl + 16 * (w & 3) + 4 * h) = cs;
        }
        f32x4 aq[2] = {zero, zero};
#pragma unroll
        for (int u = 0; u < 16; ++u) {
          aq[0] = mfma32(wqq[u], xr[u % 4][0], aq[0]);
          aq[1] = mfma32(wqq[u], xr[u % 4][1], aq[1]);
          if (u + 4 < 16) { xr[u % 4][0] = xl[((u + 4) * 2) * 64]; xr[u % 4][1] = xl[((u + 4) * 2 + 1) * 64]; }
          if (u < 12) { const int t = u >> 1; if ((u & 1) == 0) w1[2 + t] = LD1N(2 + t); else w1[18 + t] = LD1N(18 + t); }
          SB();
        }
        unsigned pq[4];
        const f32x4 csq = *(const LAS f32x4*)(csl + 16 * (w & 3) + 4 * h);
#pragma unroll
        for (int mm = 0; mm < 2; ++mm) {
          const float mean = st[16 * mm + j], rstd = st[32 + 16 * mm + j];
          float q[4];
#pragma unroll
          for (int r = 0; r < 4; ++r) q[r] = phi((aq[mm][r] - mean * csq[r]) * rstd);
          pq[2 * mm] = pk2s(q[0], q[1]); pq[2 * mm + 1] = pk2s(q[2], q[3]);
        }
        st128c(rs, v4u{pq[0], pq[1], pq[2], pq[3]}, l16, (l == 0 ? OFF_PQ0 : OFF_PQ1) + ((g * 2 + hf) * H + hd) * KB);
        SB();
      }
    }
    STAMP(1 + 6 * l);
    if (w == 4) {
      const int lane = fresh_lane();
      const int ch = lane & 7, gq = lane >> 3;
      v4u p[4]; unsigned n = 0;
      for (;;) {
#pragma unroll
        for (int i = 0; i < 4; ++i) p[i] = ld128c(rs, gq * H * KB + ch * 16, OFF_KVP + l * KVPBYTES + (i * 8 * H + g) * KB + c * 128);
        if (valid(umax(umax(umax4(p[0]), umax4(p[1])), umax(umax4(p[2]), umax4(p[3]))))) break;
        if (spin_fail(n, rs)) break;
      }
      f32x4 s = (__builtin_bit_cast(f32x4, p[0]) + __builtin_bit_cast(f32x4, p[1])) + (__builtin_bit_cast(f32x4, p[2]) + __builtin_bit_cast(f32x4, p[3]));
#pragma unroll
      for (int r = 0; r < 4; ++r) s[r] = sum_x32(sum_x16(ror_add<8>(s[r])));
      if (lane < 8) st64c(rs, v2u{pk2s(s.x, s.y), pk2s(s.z, s.w)}, lane * 16, OFF_KVR + l * KVRBYTES + ((g >> 1) * 64 + 8 * c) * 16 + (g & 1) * 8);
    } else if (w == 5 && c == 0) {
      const int lane = fresh_lane();
      const int hh = lane & 3, gq = lane >> 2;
      v4u p[2]; unsigned n = 0;
      for (;;) {
#pragma unroll
        for (int i = 0; i < 2; ++i) p[i] = ld128c(rs, gq * H * 64 + hh * 16, OFF_ZP + l * ZPBYTES + (i * 16 * H + g) * 64);
        if (valid(umax(umax4(p[0]), umax4(p[1])))) break;
        if (spin_fail(n, rs)) break;
      }
      f32x4 s = __builtin_bit_cast(f32x4, p[0]) + __builtin_bit_cast(f32x4, p[1]);
#pragma unroll
      for (int r = 0; r < 4; ++r) s[r] = sum_x32(sum_x16(ror_add<8>(ror_add<4>(s[r]))));
      if (lane < 4) st64c(rs, v2u{pk2s(s.x, s.y), pk2s(s.z, s.w)}, lane * 32, OFF_ZR + l * ZRBYTES + (g >> 2) * 128 + (g & 3) * 8);
    }
    {
      const int lane = fresh_lane(), l16 = lane * 16, h = lane >> 4;
      int sp_ = (l == 0 ? OFF_PQ0 : OFF_PQ1) + (g * 2 * H + 4 * w) * KB; OPQ(sp_);
      v4u pqa[2][4];
      __builtin_amdgcn_s_sleep(PQ_SLEEP);
#pragma unroll
      for (int hh = 0; hh < 4; ++hh) { pqa[0][hh] = ld128c(rs, l16 + hh * KB, sp_); pqa[1][hh] = ld128c(rs, l16 + hh * KB, sp_ + H * KB); }
      SB();
      if (w == 7) {
        v4u t[17];
        unsigned n = 0;
        for (;;) {
#pragma unroll
          for (int i = 0; i < 16; ++i) t[i] = ld128c(rs, l16 + (i & 3) * KB, OFF_KVR + l * KVRBYTES + (i >> 2) * 4 * KB);
          t[16] = ld128c(rs, l16, OFF_ZR + l * ZRBYTES);
          unsigned m = 0;
#pragma unroll
          for (int i = 0; i < 17; ++i) m = umax(m, umax4(t[i]));
          if (valid(m)) break;
          if (spin_fail(n, rs)) break;
        }
#pragma unroll
        for (int i = 0; i < 17; ++i) *(LAS v4u*)(L + LDS_KV + i * KB + l16) = t[i];
      }
      STAMP(2 + 6 * l);
      __syncthreads();
      v4u kvf[2], zf[2];
      kvf[0] = *(const LAS v4u*)(L + LDS_KV + 2 * w * KB + l16);
      kvf[1] = *(const LAS v4u*)(L + LDS_KV + (2 * w + 1) * KB + l16);
      zf[0] = *(const LAS v4u*)(L + LDS_KV + 16 * KB + w * 128 + h * 32);
      zf[1] = *(const LAS v4u*)(L + LDS_KV + 16 * KB + w * 128 + h * 32 + 16);
      const int x1o = OFF_X1 + 2 * l * XBYTES + g * 64 * KB, s1o = OFF_ST1 + 2 * l * STBYTES + g * 8 * KB;
#pragma unroll
      for (int hf = 0; hf < 2; ++hf) {
        v4u (&pq)[4] = pqa[hf];
        if (__builtin_expect(!valid(umax(umax(umax4(pq[0]), umax4(pq[1])), umax(umax4(pq[2]), umax4(pq[3])))), 0)) {
          unsigned n = 0;
          do {
            if (spin_fail(n, rs)) break;
#pragma unroll
            for (int hh = 0; hh < 4; ++hh) reld128(pq[hh], ws, l16 + hh * KB + sp_ + hf * H * KB);
            asm volatile("s_waitcnt vmcnt(0)" : "+v"(pq[0]), "+v"(pq[1]), "+v"(pq[2]), "+v"(pq[3]) :: "memory");
          } while (!valid(umax(umax(umax4(pq[0]), umax4(pq[1])), umax(umax4(pq[2]), umax4(pq[3])))));
        }
        v4u of[2][2];
#pragma unroll
        for (int hp = 0; hp < 2; ++hp) {
          f32x4 num[2][2], den[2][2];
#pragma unroll
          for (int t = 0; t < 2; ++t) {
            const v4u p = pq[hp * 2 + t];
            const v2u ka = t ? v2u{kvf[hp].z, kvf[hp].w} : v2u{kvf[hp].x, kvf[hp].y};
            const v2u za = t ? v2u{zf[hp].z, zf[hp].w} : v2u{zf[hp].x, zf[hp].y};
            num[t][0] = mfma16(ka, v2u{p.x, p.y}, zero); den[t][0] = mfma16(za, v2u{p.x, p.y}, zero);
            num[t][1] = mfma16(ka, v2u{p.z, p.w}, zero); den[t][1] = mfma16(za, v2u{p.z, p.w}, zero);
          }
          SB();
#pragma unroll
          for (int mm = 0; mm < 2; ++mm) {
            const float r0 = __builtin_amdgcn_rcpf(den[0][mm].x + 1e-6f), r1 = __builtin_amdgcn_rcpf(den[1][mm].x + 1e-6f);
            of[hp][mm] = v4u{pk2(num[0][mm].x * r0, num[0][mm].y * r0), pk2(num[0][mm].z * r0, num[0][mm].w * r0),
                             pk2(num[1][mm].x * r1, num[1][mm].y * r1), pk2(num[1][mm].z * r1, num[1][mm].w * r1)};
          }
          SB();
        }
        SB();
        if (X1_POS == 0 && hf == 1) x_issue(xs, rs, OFF_X1 + 2 * l * XBYTES + sx_, OFF_ST1 + 2 * l * STBYTES + ss_);
        if (ROLE_ORD && G == 1 && W1_G1) {
          if (hf == 1) {
#pragma unroll
            for (int u = 8; u < 16; ++u) { w1[u] = LD1N(u); w1[16 + u] = LD1N(16 + u); }
          }
        } else {
#pragma unroll
          for (int u = 8 + 4 * hf; u < 12 + 4 * hf; ++u) { w1[u] = LD1N(u); w1[16 + u] = LD1N(16 + u); }
        }
        LAS f32x4* red = (LAS f32x4*)(L + hf * LDS_REG + w * 8 * KB + l16);
#pragma unroll
        for (int nt = 0; nt < 4; ++nt) {
#pragma unroll
          for (int mm = 0; mm < 2; ++mm) {
            f32x4 acc = mfma32(wo[nt * 2], of[0][mm], zero);
            acc = mfma32(wo[nt * 2 + 1], of[1][mm], acc);
            red[(nt * 2 + mm) * 64] = acc;
          }
        }
        __syncthreads();
        if (X1_POS == 1 && hf == 1) x_issue(xs, rs, OFF_X1 + 2 * l * XBYTES + sx_, OFF_ST1 + 2 * l * STBYTES + ss_);
        red_out<false>(rsk, rsk, L, hf, x1o + hf * 32 * KB, s1o + hf * 4 * KB, g, c, hf, w);
        if (X1_POS == 2 && hf == 1) x_issue(xs, rs, OFF_X1 + 2 * l * XBYTES + sx_, OFF_ST1 + 2 * l * STBYTES + ss_);
      }
    }
    STAMP(3 + 6 * l);
    v4u hfr[14];
    int sh_ = OFF_H + l * HBYTES + ((g * 2 * 56 + 7 * w) * 2) * KB; OPQ(sh_);
    {
      const int xo = OFF_X1 + 2 * l * XBYTES + sx_, so = OFF_ST1 + 2 * l * STBYTES + ss_;
      if (l == 0) {
        f32x4 ca = zero, cb = zero;
#pragma unroll
        for (int u = 0; u < 16; ++u) { ca = mfma32(w1[u], ones4, ca); cb = mfma32(w1[16 + u], ones4, cb); }
        const int ln = fresh_lane();
        if ((ln & 15) == 0) { *(LAS f32x4*)(csl + 192 + 32 * w + 4 * (ln >> 4)) = ca; *(LAS f32x4*)(csl + 192 + 32 * w + 16 + 4 * (ln >> 4)) = cb; }
      }
#pragma unroll
      for (int hf = 0; hf < 2; ++hf) {
        x_finish(xs, rs, ws, xo + hf * 32 * KB, so + hf * 4 * KB, L, hf, w);
        const int lane = fresh_lane(), l16 = lane * 16, j = lane & 15, h = lane >> 4;
        const LAS v4u* xl = (const LAS v4u*)(L + hf * LDS_REG + l16);
        const LAS float* st = (const LAS float*)(L + LDS_STAT + hf * 256);
        f32x4 a0[2] = {zero, zero}, a1[2] = {zero, zero};
        v4u xr[3][2];
#pragma unroll
        for (int u = 0; u < 3; ++u) { xr[u][0] = xl[(u * 2) * 64]; xr[u][1] = xl[(u * 2 + 1) * 64]; }
        constexpr int W2S = W2_EARLY + ((ROLE_ORD && G == 1) ? W2X : 0);
        if (ROLE_ORD && G == 1 && hf == 0) {
#pragma unroll
          for (int i = 0; i < W2_EARLY; ++i) w2[i] = LD2(i);
        }
        if (ROLE_ORD && G == 1 && hf == 1) {
#pragma unroll
          for (int i = W2_EARLY; i < W2S; ++i) w2[i] = LD2(i);
        }
        SB();
#pragma unroll
        for (int u = 0; u < 16; ++u) {
          const v4u x0 = xr[u % 3][0], x1 = xr[u % 3][1];
          a0[0] = mfma32(w1[u], x0, a0[0]); a1[0] = mfma32(w1[16 + u], x0, a1[0]);
          a0[1] = mfma32(w1[u], x1, a0[1]); a1[1] = mfma32(w1[16 + u], x1, a1[1]);
          if (u + 3 < 16) { xr[u % 3][0] = xl[((u + 3) * 2) * 64]; xr[u % 3][1] = xl[((u + 3) * 2 + 1) * 64]; }
          if (hf == 0) { if (!ROLE_ORD && (u & 1) == 0 && W2_WO + (u >> 1) < W2_EARLY) w2[W2_WO + (u >> 1)] = LD2(W2_WO + (u >> 1)); }
          else { const int i0 = W2S + ((28 - W2S) * u) / 16, i1 = W2S + ((28 - W2S) * (u + 1)) / 16;
#pragma unroll
                 for (int i = i0; i < i1; ++i) w2[i] = LD2(i); }
          if (hf == 0 && u == XI_U) x_issue(xs, rs, xo + 32 * KB, so + 4 * KB);
          SB();
        }
        if (hf == 0 && XI_U == 16) x_issue(xs, rs, xo + 32 * KB, so + 4 * KB);
        if (ROLE_ORD && G == 0 && hf == 0) {
#pragma unroll
          for (int i = 0; i < W2_EARLY; ++i) w2[i] = LD2(i);
        }
        if (hf == 1) {
#pragma unroll
          for (int i = 0; i < 14; ++i) hfr[i] = LDH(i);
        }
        SB();
        {
          const f32x4 c0 = *(const LAS f32x4*)(csl + 192 + 32 * w + 4 * h), c1 = *(const LAS f32x4*)(csl + 192 + 32 * w + 16 + 4 * h);
#pragma unroll
          for (int mm = 0; mm < 2; ++mm) {
            const float mean = st[16 * mm + j], rstd = st[32 + 16 * mm + j];
            float t[8];
#pragma unroll
            for (int r = 0; r < 4; ++r) { t[r] = (a0[mm][r] - mean * c0[r]) * rstd; t[4 + r] = (a1[mm][r] - mean * c1[r]) * rstd; }
#pragma unroll
            for (int r = 0; r < 8; ++r) t[r] = t[r] * __builtin_amdgcn_rcpf(1.f + fexp(-t[r]));
            st128c(rs1, v4u{pk2s(t[0], t[1]), pk2s(t[2], t[3]), pk2s(t[4], t[5]), pk2s(t[6], t[7])}, l16,
                   OFF_H + l * HBYTES + (((g * 2 + hf) * 56 + 7 * c + w) * 2 + mm) * KB);
          }
        }
        if (FILL_MAIN && G == 0 && hf == 0) fill_group(__builtin_amdgcn_make_buffer_rsrc((void*)ws, 0, l == 0 ? WS_TOTAL : 0, 0x00020000), 1);
        SB();
      }
    }
    STAMP(4 + 6 * l);
    {
      const rsrc_t rsn = __builtin_amdgcn_make_buffer_rsrc((void*)ws, 0, l == 0 ? WS_TOTAL : 0, 0x00020000);
      const rsrc_t rsx = __builtin_amdgcn_make_buffer_rsrc((void*)ws, 0, (l == 0 && w < 4) ? WS_TOTAL : 0, 0x00020000);
      const rsrc_t rso = __builtin_amdgcn_make_buffer_rsrc((void*)out, 0, (l == 1 && w < 4) ? S * D * 2 : 0, 0x00020000);
      const int x2o = OFF_X1 + XBYTES + g * 64 * KB, s2o = OFF_ST1 + STBYTES + g * 8 * KB;
#pragma unroll
      for (int hf = 0; hf < 2; ++hf) {
        const int l16 = fresh_lane() * 16;
        if (__builtin_expect(!valid(hmax(hfr)), 0)) {
          unsigned n = 0;
          do {
            if (spin_fail(n, rs)) break;
            unsigned vb = (unsigned)(fresh_lane() * 16 + sh_ + hf * 112 * KB);
#pragma unroll
            for (int i = 0; i < 14; ++i) { reld128(hfr[i], ws, vb); vb += KB; asm volatile("" : "+v"(vb)); }
            asm volatile("s_waitcnt vmcnt(0)" : "+v"(hfr[0]), "+v"(hfr[1]), "+v"(hfr[2]), "+v"(hfr[3]), "+v"(hfr[4]), "+v"(hfr[5]), "+v"(hfr[6]),
                         "+v"(hfr[7]), "+v"(hfr[8]), "+v"(hfr[9]), "+v"(hfr[10]), "+v"(hfr[11]), "+v"(hfr[12]), "+v"(hfr[13]) :: "memory");
          } while (!valid(hmax(hfr)));
        }
        if (hf == 0) STAMP(5 + 6 * l);
        f32x4 acc[4][2];
#pragma unroll
        for (int nt = 0; nt < 4; ++nt) { acc[nt][0] = zero; acc[nt][1] = zero; }
#pragma unroll
        for (int u = 0; u < 7; ++u) {
#pragma unroll
          for (int nt = 0; nt < 4; ++nt) {
            acc[nt][0] = mfma32(w2[u * 4 + nt], hfr[u * 2], acc[nt][0]);
            acc[nt][1] = mfma32(w2[u * 4 + nt], hfr[u * 2 + 1], acc[nt][1]);
          }
          if (hf == 1) {
#pragma unroll
            for (int i = (WKV_LOOP * u) / 7; i < (WKV_LOOP * (u + 1)) / 7; ++i) wkv[i] = LDK(rsn, i);
          }
          SB();
        }
        if (hf == 0) {
        } else {
          x_issue(xs, rsn, OFF_X1 + XBYTES + sx_, OFF_ST1 + STBYTES + ss_);
        }
        red_write(acc, L, hf, w);
        __syncthreads();
        if (hf == 0) {
#pragma unroll
          for (int i = 0; i < 14; ++i) hfr[i] = LDH(112 + i);
        }
        red_out<true>(rsx, rso, L, hf, x2o + hf * 32 * KB, s2o + hf * 4 * KB, g, c, hf, w);
        if (hf == 1) {
#pragma unroll
          for (int i = WKV_LOOP; i < 16; ++i) wkv[i] = LDK(rsn, i);
        }
      }
    }
    STAMP(6 + 6 * l);
  }
  STAMP_FLUSH;
}
__global__ __launch_bounds__(512, 2) void k_main(unsigned char* ws, const uint16_t* xin, uint16_t* out) {
  extern __shared__ __attribute__((aligned(16))) unsigned char smem[];
  LAS unsigned char* L = (LAS unsigned char*)smem;
  {
    const unsigned char* pc = (const unsigned char*)__builtin_amdgcn_s_getpc();
    const int wv = __builtin_amdgcn_readfirstlane(threadIdx.x >> 6);
#pragma unroll
    for (int i = 0; i < CODE_PF / 8; ++i)
      __builtin_amdgcn_global_load_lds((const GAS void*)(pc + (size_t)(i * 8 + wv) * 1024 + (threadIdx.x & 63) * 16), (LAS void*)(L + LDS_REG + 32 * KB + wv * KB), 16, 0, 0);
  }
  if (__builtin_amdgcn_readfirstlane(threadIdx.x >> 6) < 4) run<0>(ws, xin, out, L);
  else run<1>(ws, xin, out, L);
}
}

extern "C" void kernel_launch(void* const* d_in, const int* in_sizes, int n_in, void* d_out, int out_size, void* d_ws, size_t ws_size, hipStream_t stream) {
  const uint16_t* x = (const uint16_t*)d_in[0];
  const uint16_t* wqkv = (const uint16_t*)d_in[1];
  const uint16_t* wo = (const uint16_t*)d_in[2];
  const uint16_t* w1 = (const uint16_t*)d_in[4];
  const uint16_t* w2 = (const uint16_t*)d_in[5];
  unsigned char* ws = (unsigned char*)d_ws;
  uint16_t* out = (uint16_t*)d_out;
  (void)hipFuncSetAttribute((const void*)k_main, hipFuncAttributeMaxDynamicSharedMemorySize, LDS_BYTES);
  k_prep<<<PREP_BLOCKS, 256, 0, stream>>>(wqkv, wo, w1, w2, ws);
  k_main<<<256, 512, LDS_BYTES, stream>>>(ws, x, out);
}
```
